```python
import torch

def _softmax(x, axis=-1):
    return torch.softmax(x, dim=axis)
def _gelu(x):
    return torch.nn.functional.gelu(x, approximate="tanh")
def _silu(x):
    return torch.nn.functional.silu(x)
def _sigmoid(x):
    return torch.sigmoid(x)
def _layernorm(x, axis=-1, eps=1e-5):
    m = x.mean(dim=axis, keepdim=True)
    v = x.var(dim=axis, keepdim=True, unbiased=False)
    return (x - m) / torch.sqrt(v + eps)
def _scatter_add(tgt, idx, src, axis=0):
    return tgt.clone().index_add_(axis, idx, src)
def _segment_sum(src, seg, n):
    out = torch.zeros((n,) + src.shape[1:], dtype=src.dtype)
    return out.index_add_(0, seg, src)
def _pad(x, pw):
    flat = tuple(v for pair in reversed(pw) for v in pair)
    return torch.nn.functional.pad(x, flat)

kernel_name = 'gated_mix__ssm_conv_s4d__mha_block__prompt'

def setup_inputs():

    _w = (lambda a: (lambda t: t.to(torch.bfloat16) if t.is_floating_point() else t)(torch.as_tensor(a)))
    def _A_setup():
        Bsz, L, D, N = 1, 2048, 576, 128
        return {
            "u": _w((lambda *s: torch.randn(*s, dtype=torch.bfloat16) * 0.05)(Bsz, L, D)),
            "log_A_real": _w((lambda *s: torch.randn(*s, dtype=torch.bfloat16) * 6.4)(D, N) * 0.5),
            "A_imag": _w((lambda *s: torch.randn(*s, dtype=torch.bfloat16) * 0.1)(D, N) * 3.14),
            "C_real": _w((lambda *s: torch.randn(*s, dtype=torch.bfloat16) * 0.4)(D, N) / N ** 0.5),
            "C_imag": _w((lambda *s: torch.randn(*s, dtype=torch.bfloat16) * 0.4)(D, N) / N ** 0.5),
            "log_dt": _w((lambda *s: torch.randn(*s, dtype=torch.bfloat16) * 0.1)(D) * 0.5 - 4.0),
            "D_skip": _w((lambda *s: torch.randn(*s, dtype=torch.bfloat16) * 0.1)(D)),
        }
    _A_in = _A_setup()

    def _B_setup():
        B, S, D, H = 1, 2048, 576, 32
        return {
            "x": _w((lambda *s: torch.randn(*s, dtype=torch.bfloat16) * 0.05)(B, S, D)),
            "wqkv": _w((lambda *s: torch.randn(*s, dtype=torch.bfloat16) * 0.8)(D, 3 * D)),
            "wo": _w((lambda *s: torch.randn(*s, dtype=torch.bfloat16) * 0.05)(D, D)),
        }
    _B_in = _B_setup()

    return {
        "A_u": _A_in["u"],
        "A_log_A_real": _A_in["log_A_real"],
        "A_A_imag": _A_in["A_imag"],
        "A_C_real": _A_in["C_real"],
        "A_C_imag": _A_in["C_imag"],
        "A_log_dt": _A_in["log_dt"],
        "A_D_skip": _A_in["D_skip"],
        "B_x": _B_in["x"],
        "B_wqkv": _B_in["wqkv"],
        "B_wo": _B_in["wo"],
        "gate_w": _w((lambda *s: torch.randn(*s, dtype=torch.bfloat16) * 0.05)(576) * 16.0),
    }

def reference(A_u, A_log_A_real, A_A_imag, A_C_real, A_C_imag, A_log_dt, A_D_skip, B_x, B_wqkv, B_wo, gate_w):
    def _A_ref(u, log_A_real, A_imag, C_real, C_imag, log_dt, D_skip):
        L = u.shape[1]
        dt = torch.exp(log_dt)
        A = -torch.exp(log_A_real) + 1j * A_imag
        C = (C_real + 1j * C_imag) * dt[:, None]
        pos = (lambda x, dt: x.to(dt))(torch.arange(L), log_dt.dtype)
        P = torch.exp((A * dt[:, None])[:, :, None] * pos[None, None, :])
        K = 2.0 * torch.einsum("dn,dnl->dl", C, P).real
        L2 = 2 * L
        u_t = (lambda x, *d: x.permute(*d))(u, 0, 2, 1)
        Kf = (lambda x, *a, **k: torch.fft.rfft(x.float(), *a, **k))(K, L2)
        Uf = (lambda x, *a, **k: torch.fft.rfft(x.float(), *a, **k))(u_t, L2)
        y = (lambda x, *a, **k: torch.fft.irfft(x, *a, **k).to(torch.bfloat16))(Uf * Kf[None, :, :], L2)[:, :, :L]
        y = (lambda x, *d: x.permute(*d))(y, 0, 2, 1)
        return y + u * D_skip[None, None, :]

    def _B_ref(x, wqkv, wo):
        B, S, D = x.shape
        H = 32
        Dh = D // H
        qkv = (lambda x, *d: x.permute(*d))((x @ wqkv).reshape(B, S, 3, H, Dh), 2, 0, 3, 1, 4)
        q, k, v = qkv[0], qkv[1], qkv[2]
        scores = torch.einsum("bhqd,bhkd->bhqk", q, k) * (Dh ** -0.5)
        p = _softmax(scores, axis=-1)
        o = torch.einsum("bhqk,bhkd->bhqd", p, v)
        return (lambda x, *d: x.permute(*d))(o, 0, 2, 1, 3).reshape(B, S, D) @ wo

    x = A_u
    g = _sigmoid((x * gate_w).sum(-1, keepdims=True))
    a = _A_ref(x, A_log_A_real, A_A_imag, A_C_real, A_C_imag, A_log_dt, A_D_skip)
    b = _B_ref(x, B_wqkv, B_wo)
    return g * a + (1 - g) * b
```

```cpp
#if defined(__HIP_DEVICE_COMPILE__)
#pragma clang attribute push(__attribute__((target("no-packed-fp32-ops"))), apply_to = function)
#endif
#include <hip/hip_runtime.h>
#include <stdint.h>

typedef short bf16x8 __attribute__((ext_vector_type(8)));
typedef short bf16x4 __attribute__((ext_vector_type(4)));
typedef _Float16 f16x8 __attribute__((ext_vector_type(8)));
typedef _Float16 f16x2 __attribute__((ext_vector_type(2)));
typedef __bf16 bf16x2v __attribute__((ext_vector_type(2)));
typedef float f32x2 __attribute__((ext_vector_type(2)));
typedef float f32x4 __attribute__((ext_vector_type(4)));
typedef float f32x16 __attribute__((ext_vector_type(16)));
typedef unsigned u32x2 __attribute__((ext_vector_type(2)));
typedef unsigned u32x4 __attribute__((ext_vector_type(4)));
#define TIDX ((int)__builtin_amdgcn_workitem_id_x())
#define BIDX ((int)__builtin_amdgcn_workgroup_id_x())
#define LAS __attribute__((address_space(3)))
#define DI __device__ __forceinline__

constexpr int SEQ = 2048, DM = 576, NH = 32, DH = 18, NMODE = 128;

constexpr int KV_TILE = 2560;
constexpr size_t WS_KV = 0;
constexpr size_t WS_QM = 5248 * 1024;
constexpr size_t WS_QE = WS_QM + 2048 * 1024;
constexpr size_t WS_KMAX = WS_QE + 512 * 1024;
constexpr size_t WS_GATE = WS_KMAX + 4096;
constexpr size_t WS_UT = WS_GATE + 8192;
constexpr int KR_ROW = 4192;
constexpr size_t WS_KR = WS_UT + 576 * 4096;
constexpr size_t WS_YT = WS_KR + 576 * KR_ROW;
constexpr size_t WS_OF = WS_YT + 576 * 4096;
constexpr size_t WS_CNT = WS_OF + 128 * 18 * 1024;
constexpr size_t WS_END = WS_CNT + 4096;
constexpr unsigned O_UNSET = 0xffffffffu;
static_assert(NH * 64 * KV_TILE + KV_TILE <= WS_QM, "kv region");

struct Params {
    const unsigned short *x, *logA, *Aim, *Cre, *Cim, *logdt, *dskip, *wqkv, *wo, *gatew;
    unsigned short* out;
    char* ws;
};

DI float bf2f(unsigned short b) { return __uint_as_float((unsigned)b << 16); }
DI float bflo(unsigned u) { return __uint_as_float(u << 16); }
DI float bfhi(unsigned u) { return __uint_as_float(u & 0xffff0000u); }
DI unsigned pkbf(float lo, float hi) { f32x2 v = {lo, hi}; return __builtin_bit_cast(unsigned, __builtin_convertvector(v, bf16x2v)); }
DI unsigned pkh(float lo, float hi) { f32x2 v = {lo, hi}; return __builtin_bit_cast(unsigned, __builtin_convertvector(v, f16x2)); }
DI float bfround(float v) { return bflo(pkbf(v, 0.f) & 0xffffu); }
DI float dot2bf(unsigned a, unsigned b, float c) { return __builtin_amdgcn_fdot2_f32_bf16(__builtin_bit_cast(bf16x2v, a), __builtin_bit_cast(bf16x2v, b), c, false); }
DI int rfl(int v) { return __builtin_amdgcn_readfirstlane(v); }
DI void dma16(unsigned voff, const void* sbase, unsigned lds_base) {
    asm volatile("s_mov_b32 m0, %2\n\ts_nop 0\n\tglobal_load_lds_dwordx4 %0, %1" :: "v"(voff), "s"(sbase), "s"(lds_base) : "memory");
}
DI void dma16m(unsigned voff, const void* sbase, unsigned lds_base, unsigned long long mask) {
    asm volatile("s_mov_b32 m0, %2\n\ts_mov_b64 exec, %3\n\tglobal_load_lds_dwordx4 %0, %1\n\ts_mov_b64 exec, -1"
                 :: "v"(voff), "s"(sbase), "s"(lds_base), "s"(mask) : "memory");
}
DI void code_prefetch(unsigned long long entry, int chunk, unsigned lds_scratch, int lane) {
    asm volatile("s_mov_b32 m0, %2\n\ts_nop 0\n\tglobal_load_lds_dword %0, %1" :: "v"(lane * 64), "s"(entry + chunk * 4096), "s"(lds_scratch) : "memory");
}
DI void touch_lines(const void* base, unsigned voff, unsigned lds_scratch) {
    asm volatile("s_mov_b32 m0, %2\n\ts_nop 0\n\tglobal_load_lds_dword %0, %1" :: "v"(voff), "s"(base), "s"(lds_scratch) : "memory");
}
DI unsigned long long code_entry() { unsigned long long pc; asm volatile("s_getpc_b64 %0" : "=s"(pc)); return pc & ~255ull; }
DI unsigned wave_max_u32(unsigned m) {
    m = max(m, (unsigned)__builtin_amdgcn_update_dpp(0, (int)m, 0x111, 0xf, 0xf, true));
    m = max(m, (unsigned)__builtin_amdgcn_update_dpp(0, (int)m, 0x112, 0xf, 0xf, true));
    m = max(m, (unsigned)__builtin_amdgcn_update_dpp(0, (int)m, 0x114, 0xf, 0xf, true));
    m = max(m, (unsigned)__builtin_amdgcn_update_dpp(0, (int)m, 0x118, 0xf, 0xf, true));
    m = max(m, (unsigned)__builtin_amdgcn_update_dpp(0, (int)m, 0x142, 0xa, 0xf, true));
    m = max(m, (unsigned)__builtin_amdgcn_update_dpp(0, (int)m, 0x143, 0xc, 0xf, true));
    return m;
}
typedef short v4i16_t __attribute__((ext_vector_type(4)));
DI bf16x4 trread(LAS const char* p) { return __builtin_bit_cast(bf16x4, __builtin_amdgcn_ds_read_tr16_b64_v4i16((LAS v4i16_t*)p)); }
DI bf16x8 cat8(bf16x4 a, bf16x4 b) { return __builtin_shufflevector(a, b, 0, 1, 2, 3, 4, 5, 6, 7); }
DI void st16(void* p, u32x4 v) { asm volatile("global_store_dwordx4 %0, %1, off sc1\n\ts_nop 1" :: "v"(p), "v"(v) : "memory"); }
DI void st8(void* p, u32x2 v) { asm volatile("global_store_dwordx2 %0, %1, off sc1" :: "v"(p), "v"(v) : "memory"); }
DI void st4(void* p, unsigned v) { asm volatile("global_store_dword %0, %1, off sc1" :: "v"(p), "v"(v) : "memory"); }
constexpr int G_XST = 16384, G_WST = 7168, G_WOFF = 3 * G_XST, G_GW = G_WOFF + 4 * G_WST;
constexpr int K1_LDS = 79872;

constexpr int STG_ROW = 232;
constexpr float QSCALE = 0.34004498f;
#define MFMA32(a, b, c) __builtin_amdgcn_mfma_f32_32x32x16_bf16(a, b, c, 0, 0, 0)

constexpr int gemm_inflight(int ks) {
    int pos = 0, need = 0;
    auto X = [&](int i) { pos += 4; if (i == (ks >> 1)) need = pos > need ? pos : need; };
    auto W = [&](int i) { pos += 2; if (i == ks) need = pos > need ? pos : need; };
    X(0); W(0); W(1); X(1); W(2);
    for (int s = 0; s < ks; ++s) { if (s + 3 < 18) W(s + 3); if (!(s & 1) && s / 2 + 2 < 9) X(s / 2 + 2); }
    return pos - need;
}
template <int N> DI void waitbar() { asm volatile("s_waitcnt vmcnt(%0) lgkmcnt(0)\n\ts_barrier" :: "n"(N) : "memory"); }
struct GemmCtx {
    const char *x, *w;
    unsigned vx[4], vw[2];
    unsigned xdst, wdst;
    int rot, boff, aoff, goff; bool gl;
};
DI int rot9(int c, int rot) { const int v = c + rot; return v >= 9 ? v - 9 : v; }
DI void gemm_ldx(const GemmCtx& c, int xs) {
    const char* s = c.x + rot9(xs, c.rot) * 128;
#pragma unroll
    for (int i = 0; i < 4; ++i) dma16(c.vx[i], s, c.xdst + (xs % 3) * G_XST + i * 1024);
}
DI void gemm_ldw(const GemmCtx& c, int ks) {
    const char* s = c.w + (size_t)(rot9(ks >> 1, c.rot) * 2 + (ks & 1)) * (32 * 3456);
#pragma unroll
    for (int i = 0; i < 2; ++i) dma16m(c.vw[i], s, c.wdst + (ks % 4) * G_WST + i * 896, 0x00ffffffffffffffull);
}
template <int KS>
DI void gemm_produce(const GemmCtx& c) {
    if constexpr (KS < 18) {
        waitbar<gemm_inflight(KS)>();
        if constexpr (KS + 3 < 18) gemm_ldw(c, KS + 3);
        if constexpr (!(KS & 1) && KS / 2 + 2 < 9) gemm_ldx(c, KS / 2 + 2);
        gemm_produce<KS + 1>(c);
    }
}
struct GemmFrag { bf16x8 a0, a1, b0, b1; u32x4 gw; };
template <bool GATE, int KS, int KP>
DI GemmFrag gemm_read(LAS char* lds, const GemmCtx& c) {
    LAS const char* xt = lds + ((KS >> 1) % 3) * G_XST;
    LAS const char* wt = lds + (KS % 4) * G_WST + c.aoff + KP * 16 * 224;
    const int bo = c.boff ^ (KP * 32 + (KS & 1) * 64);
    GemmFrag f;
    f.a0 = cat8(trread(wt), trread(wt + 224));
    f.b0 = *(LAS const bf16x8*)(xt + bo);
    f.b1 = *(LAS const bf16x8*)(xt + bo + 32 * 128);
    f.a1 = cat8(trread(wt + 64), trread(wt + 64 + 224));
    if (GATE) f.gw = *(LAS const u32x4*)(lds + G_GW + c.goff + (rot9(KS >> 1, c.rot) * 64 + (KS & 1) * 32 + KP * 16) * 2);
    return f;
}
template <bool GATE>
DI void gemm_mma(f32x16 (&acc)[2][2], const GemmFrag& f, bool gl) {
    acc[0][0] = MFMA32(f.a0, f.b0, acc[0][0]);
    acc[0][1] = MFMA32(f.a0, f.b1, acc[0][1]);
    u32x4 u = __builtin_bit_cast(u32x4, f.a1);
    if (GATE) {
#pragma unroll
        for (int e = 0; e < 4; ++e) u[e] = gl ? f.gw[e] : u[e];
    }
    const bf16x8 a1 = __builtin_bit_cast(bf16x8, u);
    acc[1][0] = MFMA32(a1, f.b0, acc[1][0]);
    acc[1][1] = MFMA32(a1, f.b1, acc[1][1]);
}
template <bool GATE, int KS>
DI void gemm_steps(f32x16 (&acc)[2][2], LAS char* lds, const GemmCtx& c, GemmFrag q) {
    if constexpr (KS < 18) {
        asm volatile("s_waitcnt lgkmcnt(0)\n\ts_barrier" ::: "memory");
        const GemmFrag p = gemm_read<GATE, KS, 0>(lds, c);
        __builtin_amdgcn_sched_barrier(0);
        if constexpr (KS > 0) gemm_mma<GATE>(acc, q, c.gl);
        __builtin_amdgcn_sched_barrier(0);
        q = gemm_read<GATE, KS, 1>(lds, c);
        __builtin_amdgcn_sched_barrier(0);
        gemm_mma<GATE>(acc, p, c.gl);
        __builtin_amdgcn_sched_barrier(0);
        gemm_steps<GATE, KS + 1>(acc, lds, c, q);
    } else gemm_mma<GATE>(acc, q, c.gl);
}
DI void st_pair(char* p, unsigned a, unsigned b, int lane) {
    const unsigned a1 = (unsigned)__builtin_amdgcn_mov_dpp((int)a, 0xf5, 0xf, 0xf, true), b1 = (unsigned)__builtin_amdgcn_mov_dpp((int)b, 0xf5, 0xf, 0xf, true);
    if (!(lane & 1)) st16(p, u32x4{a, b, a1, b1});
}
template <int WN, bool SCALE>
DI void gemm_stage(const f32x16 (&acc)[2][2], LAS char* rowp, int hh, int qlim) {
#pragma unroll
    for (int m = 0; m < 2; ++m)
#pragma unroll
        for (int q = 0; q < 4; ++q) {
            const int n0 = 64 * WN + 32 * m + 8 * q;
            if (n0 < 108) {
                const float sc = (SCALE && n0 < qlim) ? QSCALE : 1.0f;
#pragma unroll
                for (int tt = 0; tt < 2; ++tt) {
                    const f32x16& a = acc[m][tt];
                    const u32x2 v = SCALE ? u32x2{pkbf(a[4 * q] * sc, a[4 * q + 1] * sc), pkbf(a[4 * q + 2] * sc, a[4 * q + 3] * sc)}
                                          : u32x2{pkbf(a[4 * q], a[4 * q + 1]), pkbf(a[4 * q + 2], a[4 * q + 3])};
                    *(LAS u32x2*)(rowp + tt * 32 * STG_ROW + n0 * 2) = v;
                }
            }
        }
}

DI void gemm_role(const Params& p, LAS char* lds, int b) {
    const int tid = TIDX, lane = tid & 63, wave = rfl(tid >> 6);

    const int xcd = b & 7, idx = b >> 3;
    const int rb = 4 * (xcd >> 1) + (idx & 3), cb = 8 * (xcd & 1) + (idx >> 2);
    const int t0 = rb * 128;
    const unsigned ldsb = (unsigned)(size_t)lds;
    GemmCtx c;
    c.rot = idx % 9;
    const int r = lane & 31, hh = lane >> 5;
    if (wave >= 4) {
        const int pw = wave - 4;
        c.x = (const char*)p.x + (size_t)t0 * 1152;
        c.w = (const char*)p.wqkv + ((216 * cb) & ~15);
        c.xdst = ldsb + pw * 4096; c.wdst = ldsb + G_WOFF + pw * 1792;
        if (pw == 3) { dma16(lane * 16, (const char*)p.gatew, ldsb + G_GW); dma16m(lane * 16, (const char*)p.gatew + 1024, ldsb + G_GW + 1024, 0xffull); }
#pragma unroll
        for (int i = 0; i < 4; ++i) {
            const int xrow = 32 * pw + 8 * i + (lane >> 3);
            c.vx[i] = xrow * 1152 + (((lane & 7) ^ ((xrow >> 1) & 7)) << 4);
        }
        gemm_ldx(c, 0);
        __builtin_amdgcn_sched_barrier(0);
        const int wl = lane < 56 ? lane : 0, rq = (wl * 4682) >> 16;
#pragma unroll
        for (int i = 0; i < 2; ++i) {
            const int rho = 8 * pw + 4 * i + rq, kr = (rho & 24) | ((rho & 1) << 2) | ((rho >> 1) & 3);
            c.vw[i] = kr * 3456 + (wl - 14 * rq) * 16;
        }
        gemm_ldw(c, 0); gemm_ldw(c, 1); gemm_ldx(c, 1); gemm_ldw(c, 2);

        gemm_produce<0>(c);

        waitbar<0>();
    } else {
        __builtin_amdgcn_s_setprio(1);
        const int wm = wave & 1, wn = wave >> 1;
        c.boff = (64 * wm + r) * 128 + ((hh ^ ((r >> 1) & 7)) << 4);
        c.aoff = G_WOFF + (8 * hh + 2 * ((lane & 15) >> 2)) * 224 + ((216 * cb) & 15) + wn * 128 + ((lane >> 4) & 1) * 32 + (lane & 3) * 8;
        c.goff = hh * 16; c.gl = r == 12;
        f32x16 acc[2][2] = {{f32x16{}, f32x16{}}, {f32x16{}, f32x16{}}};

        if (wn == 0) gemm_steps<false, 0>(acc, lds, c, GemmFrag{}); else gemm_steps<true, 0>(acc, lds, c, GemmFrag{});

        waitbar<0>();
        LAS char* rowp = lds + (64 * wm + r) * STG_ROW + hh * 8;
        const int qlim = DM - 108 * cb - 4 * hh;
        if (cb <= 5) { if (wn == 0) gemm_stage<0, true>(acc, rowp, hh, qlim); else gemm_stage<1, true>(acc, rowp, hh, qlim); }
        else { if (wn == 0) gemm_stage<0, false>(acc, rowp, hh, qlim); else gemm_stage<1, false>(acc, rowp, hh, qlim); }
        if (wn == 1 && hh == 1 && cb == 0) {
#pragma unroll
            for (int tt = 0; tt < 2; ++tt)
                ((float*)(p.ws + WS_GATE))[t0 + 64 * wm + 32 * tt + r] = __builtin_amdgcn_rcpf(1.0f + __builtin_amdgcn_exp2f(-1.4426950408889634f * acc[1][tt][4]));
        }
    }
    waitbar<0>();

    auto v_tile = [&](int hs, int kt) {
        const int head = (6 * cb + hs) & 31, g = lane >> 4, i = lane & 15, off = 2 * (hs & 1);
        const int row = kt * 32 + (g >> 1) * 16 + (g & 1) * 4;
        LAS const char* s = lds + (row + (i >> 2)) * STG_ROW + hs * 36 - 2 * off + (lane & 3) * 8;
        const bf16x8 v0 = cat8(trread(s), trread(s + 8 * STG_ROW)), v1 = cat8(trread(s + 32), trread(s + 32 + 8 * STG_ROW));
        char* kv = p.ws + WS_KV + (size_t)(head * 64 + rb * 4 + kt) * KV_TILE + 1280 + (g * 20 + i - off) * 16;
        const unsigned c1 = i == 2 + off ? 0x3f803f80u : 0u;
        const u32x4 w1 = i < 2 + off ? __builtin_bit_cast(u32x4, v1) : u32x4{c1, c1, c1, c1};
        if (i >= off) st16(kv, __builtin_bit_cast(u32x4, v0));
        if (i < 4 + off) st16(kv + 256, w1);
    };
    if (cb >= 11) {
#pragma unroll
        for (int i = 0; i < 3; ++i) v_tile((wave + 8 * i) >> 2, wave & 3);
        return;
    }
    for (int rnd = 0; rnd < 2; ++rnd) {
        const int hs = rnd * 4 + (wave >> 1);
        if (hs >= 6) break;
        const int sec = 6 * cb + hs, type = sec >> 5, head = sec & 31, half = wave & 1;
        if (type < 2) {
            const int t = half * 64 + lane, tg = t0 + t, kt = tg >> 5;
            LAS const unsigned* s = (LAS const unsigned*)(lds + t * STG_ROW + hs * 36);
            const u32x4 a = {s[0], s[1], s[2], s[3]}, cc = {s[4], s[5], s[6], s[7]};
            const unsigned e = s[8];
            float n2 = dot2bf(e, e, 0.f), n3 = dot2bf(a[0], a[0], 0.f);
            n2 = dot2bf(a[1], a[1], n2); n3 = dot2bf(a[2], a[2], n3); n2 = dot2bf(a[3], a[3], n2); n3 = dot2bf(cc[0], cc[0], n3);
            n2 = dot2bf(cc[1], cc[1], n2); n3 = dot2bf(cc[2], cc[2], n3); n2 = dot2bf(cc[3], cc[3], n2);
            const unsigned nrm = __float_as_uint(__builtin_amdgcn_sqrtf(n2 + n3) * 1.001f);
            if (type == 0) {
                char* qm = p.ws + WS_QM + (size_t)(head * 64 + kt) * 1024 + r * 16;
                st16(qm, a); st16(qm + 512, cc);
                st_pair(p.ws + WS_QE + (size_t)(head * SEQ + tg) * 8, e, nrm, lane);
            } else {
                char* kv = p.ws + WS_KV + (size_t)(head * 64 + kt) * KV_TILE;
                st16(kv + r * 16, a); st16(kv + 512 + r * 16, cc);
                st_pair(kv + 1024 + r * 8, e, 0x3f803f80u, lane);
                const unsigned m = wave_max_u32(nrm);
                if (lane == 63) ((unsigned*)(p.ws + WS_KMAX))[head * 32 + rb * 2 + half] = m;
            }
        } else { v_tile(hs, 2 * half); v_tile(hs, 2 * half + 1); }
    }
}

DI void xt_role(const Params& p, LAS char* lds, int xcd, int task, int slot, int lane) {
    const int id = (xcd & 1) * 72 + task, tile_id = id >> 2, cg = tile_id % 9, t0 = (4 * (xcd >> 1) + tile_id / 9) * 128 + (id & 3) * 32;
    LAS char* tile = lds + 32768 + slot * 4608;
    const char* src = (const char*)p.x + (size_t)t0 * 1152 + cg * 128;
    u32x4 v[4];
#pragma unroll
    for (int i = 0; i < 4; ++i) v[i] = *(const u32x4*)(src + (8 * i + (lane >> 3)) * 1152 + (lane & 7) * 16);
#pragma unroll
    for (int i = 0; i < 4; ++i) *(LAS u32x4*)(tile + (8 * i + (lane >> 3)) * 144 + (lane & 7) * 16) = v[i];
    const int i16 = lane & 15, g = lane >> 4;
    LAS const char* s = tile + (8 * g + (i16 >> 2)) * 144 + (lane & 3) * 8;
    char* dst = p.ws + WS_UT + (size_t)(cg * 64 + i16) * 4096 + (t0 + 8 * g) * 2;
#pragma unroll
    for (int db = 0; db < 4; ++db) {
        const u32x4 o = __builtin_bit_cast(u32x4, cat8(trread(s + db * 32), trread(s + db * 32 + 4 * 144)));
        st16(dst + (size_t)db * 16 * 4096, o);
    }
}

struct KIdx { unsigned short v[2096]; };
constexpr int kd_slot(int l) {
    int sh = l < 256 ? 0 : (l < 512 ? 1 : (l < 1024 ? 2 : 3));
    int pos = l;
    if (sh) { int q = l >> sh, r = l & ((1 << sh) - 1), h = 1 << (sh - 1); if (r > h || (r == h && (q & 1))) ++q; pos = q << sh; }
    int i = 0, j = 0;
    if (pos < 256) { i = pos & 7; j = pos >> 3; }
    else if (pos < 512) { int q = pos - 256; i = 8 + ((q & 7) >> 1); j = q >> 3; }
    else if (pos < 1024) { int q = pos - 512; i = 12 + 2 * (q >> 8) + ((q & 7) >> 2); j = (q & 255) >> 3; }
    else if (pos < 2048) { int q = pos - 1024; i = 16 + (q >> 8); j = (q & 255) >> 3; }
    else { i = 20; j = 0; }
    return i * 32 + j;
}
constexpr KIdx make_kidx() { KIdx t{}; for (int x = 0; x < 2096; ++x) t.v[x] = (unsigned short)(2 * (x <= 2047 ? kd_slot(2047 - x) : 672)); return t; }
__device__ __attribute__((aligned(16))) const KIdx g_kidx = make_kidx();

struct cf { float r, i; };
DI cf cmul(cf a, cf b) { return cf{__builtin_fmaf(a.r, b.r, -(a.i * b.i)), __builtin_fmaf(a.r, b.i, a.i * b.r)}; }
DI cf cexpp(float zr2, float zrev, float pos) {
    const float e = __builtin_amdgcn_exp2f(zr2 * pos), a = __builtin_amdgcn_fractf(zrev * pos);
    return cf{e * __builtin_amdgcn_cosf(a), e * __builtin_amdgcn_sinf(a)};
}
DI float exp_c(float x) {
    const float t = x * 1.4426950216293335f, e = __builtin_fmaf(x, 1.4426950216293335f, -t) + x * 1.9259629911266175e-8f;
    const float r = __builtin_amdgcn_exp2f(t);
    return __builtin_fmaf(r, e * 0.6931471805599453f, r);
}

DI void kgen_role(const Params& p, LAS char* lds, int d, int kw, int lane) {
    unsigned short rA[2], rI[2], rCr[2], rCi[2];
#pragma unroll
    for (int h = 0; h < 2; ++h) {
        const int n = d * NMODE + h * 64 + lane;
        rA[h] = p.logA[n]; rI[h] = p.Aim[n]; rCr[h] = p.Cre[n]; rCi[h] = p.Cim[n];
    }
    const unsigned short rdt = p.logdt[d];
    u32x4 ix[5];
#pragma unroll
    for (int i = 0; i < 5; ++i) ix[i] = *(const u32x4*)&g_kidx.v[8 * min(lane + 64 * i, 261)];
    LAS char* tab = lds + kw * 8192;
    const unsigned wa = (unsigned)(size_t)tab + lane * 4;
    const int r = lane & 31, hh = lane >> 5;
    const unsigned ra = (unsigned)(size_t)tab + r * 256 + ((hh ^ (r & 15)) << 4);
    auto put = [&](int row, float re, float im) { *(LAS unsigned*)(size_t)((wa ^ ((row & 15) << 4)) + row * 256) = pkh(re, im); };
    auto putE = [&](int row, cf v) { put(row, v.r, -v.i); };
    const float dt = bfround(exp_c(bf2f(rdt)));
    float cr[2], ci[2], cm = 0.f;
#pragma unroll
    for (int h = 0; h < 2; ++h) {
        cr[h] = 2.0f * (bf2f(rCr[h]) * dt); ci[h] = 2.0f * (bf2f(rCi[h]) * dt);
        cm = __builtin_fmaxf(cm, __builtin_fmaxf(__builtin_fabsf(cr[h]), __builtin_fabsf(ci[h])));
    }
    const unsigned ce = min(max((unsigned)__builtin_amdgcn_readlane((int)wave_max_u32(__float_as_uint(cm)), 63) >> 23, 1u), 253u);
    const float scl = __uint_as_float((254u - ce) << 23) * 0.5f, inv = __uint_as_float(ce << 23) * 2.0f;

    f32x16 acc = {};
#pragma unroll 1
    for (int h = 0; h < 2; ++h) {
        const float are = -bfround(exp_c(bf2f(h ? rA[1] : rA[0])));
        const float zr2 = are * dt * 1.4426950408889634f, zrev = bf2f(h ? rI[1] : rI[0]) * dt * 0.15915494309189535f;
        const cf C = cf{(h ? cr[1] : cr[0]) * scl, (h ? ci[1] : ci[0]) * scl};
        {
            const cf w1 = cexpp(zr2, zrev, 1.f), w2 = cmul(w1, w1), w4 = cmul(w2, w2), w6 = cmul(w4, w2);
            const cf w256 = cexpp(zr2, zrev, 256.f), w512 = cexpp(zr2, zrev, 512.f);
            cf e = C;
            putE(0, e); e = cmul(e, w1); putE(1, e);
            cf e2 = cmul(C, w2);
            putE(2, e2); putE(3, cmul(e2, w1));
            const cf c4 = cmul(C, w4);
            putE(4, c4); putE(5, cmul(c4, w1));
            e2 = cmul(c4, w2);
            putE(6, e2); putE(7, cmul(e2, w1));
            const cf a256 = cmul(C, w256);
            putE(8, a256); putE(9, cmul(a256, w2)); putE(10, cmul(a256, w4)); putE(11, cmul(a256, w6));
            const cf a512 = cmul(C, w512), a768 = cmul(a512, w256);
            putE(12, a512); putE(13, cmul(a512, w4)); putE(14, a768); putE(15, cmul(a768, w4));
            const cf a1024 = cmul(C, cexpp(zr2, zrev, 1024.f)), a1536 = cmul(a1024, w512);
            putE(16, a1024); putE(17, cmul(a1024, w256)); putE(18, a1536); putE(19, cmul(a1536, w256));
            putE(20, cmul(C, cexpp(zr2, zrev, 2048.f)));
        }
        f16x8 ea[8];
#pragma unroll
        for (int k = 0; k < 8; ++k) ea[k] = *(LAS const f16x8*)(size_t)(ra ^ (k << 5));
        asm volatile("s_waitcnt lgkmcnt(0)" ::: "memory");
        {
            const cf w8 = cexpp(zr2, zrev, 8.f), w16 = cmul(w8, w8), w24 = cmul(w16, w8), w32 = cmul(w16, w16);
            cf f0 = cf{1.f, 0.f}, f1 = w8, f2 = w16, f3 = w24;
#pragma unroll
            for (int q = 0; q < 8; ++q) {
                put(4 * q, f0.r, f0.i); put(4 * q + 1, f1.r, f1.i); put(4 * q + 2, f2.r, f2.i); put(4 * q + 3, f3.r, f3.i);
                if (q == 0) f0 = w32; else if (q < 7) f0 = cmul(f0, w32);
                if (q < 7) { f1 = cmul(f1, w32); f2 = cmul(f2, w32); f3 = cmul(f3, w32); }
            }
        }
#pragma unroll
        for (int k = 0; k < 8; ++k) {
            const f16x8 fb = *(LAS const f16x8*)(size_t)(ra ^ (k << 5));
            acc = __builtin_amdgcn_mfma_f32_32x32x16_f16(ea[k], fb, acc, 0, 0, 0);
        }
        asm volatile("s_waitcnt lgkmcnt(0)" ::: "memory");
    }

#pragma unroll
    for (int i = 0; i < 12; ++i) {
        const int row = (i & 3) + 8 * (i >> 2) + 4 * hh;
        if (row < 21) *(LAS unsigned short*)(tab + (row * 32 + r) * 2) = (unsigned short)(pkbf(acc[i] * inv, 0.f) & 0xffffu);
    }
    if (lane == 0) *(LAS unsigned short*)(tab + 672 * 2) = 0;

    char* kr = p.ws + WS_KR + (size_t)d * KR_ROW;
#pragma unroll
    for (int i = 0; i < 5; ++i) {
        const int c = lane + 64 * i;
        unsigned v[8];
#pragma unroll
        for (int e = 0; e < 8; ++e) v[e] = *(LAS const unsigned short*)(tab + ((ix[i][e >> 1] >> (16 * (e & 1))) & 0xffffu));
        if (c < 262) st16(kr + c * 16, u32x4{v[0] | v[1] << 16, v[2] | v[3] << 16, v[4] | v[5] << 16, v[6] | v[7] << 16});
    }
}

__global__ __launch_bounds__(512, 4) void k1_proj(Params p) {
    extern __shared__ __attribute__((aligned(16))) char smem[];
    LAS char* lds = (LAS char*)smem;
    const int b = BIDX;
    const unsigned long long entry = code_entry();
    if (b < 8) {
        const int w = rfl(TIDX >> 6), l = TIDX & 63;
        code_prefetch(entry, w, (unsigned)(size_t)lds + 66048, l);
        if (w == 0 || (w == 1 && l < 32)) code_prefetch(entry, 8 + w, (unsigned)(size_t)lds + 66048, l);
    }
    if (b < 256) {
        const int lane = TIDX & 63, wave = rfl(TIDX >> 6), idx = b >> 3;

        if (b == 0 && TIDX < 16) ((unsigned*)(p.ws + WS_CNT))[TIDX * 64] = 0u;
        const int nk = b < 64 ? 3 : 2, c0 = b < 64 ? 3 * b : 192 + 2 * (b - 64);
        const int nx = idx < 24 ? 2 : 3, j0 = idx < 24 ? 2 * idx : 48 + 3 * (idx - 24);
        const int xs = wave == 3 ? 0 : (wave == 7 ? 1 : (wave == 6 ? 2 : 3));
        if (wave < nk) kgen_role(p, lds, c0 + wave, wave, lane);
        else if (xs < nx) xt_role(p, lds, b & 7, j0 + xs, xs, lane);
        else if (wave == 4 || wave == 5) {
            if (wave == 4 && b < 162) touch_lines((const char*)p.wo + b * 4096, lane * 64, (unsigned)(size_t)lds + 65536);
            char* po = p.ws + WS_OF + (size_t)(b >> 5) * 294912 + (b & 31) * 4608 + (wave - 4) * 3072 + lane * 16;
#pragma unroll
            for (int i = 0; i < 3; ++i)
                if (wave == 4 || i * 64 + lane < 96) st16(po + i * 1024, u32x4{O_UNSET, O_UNSET, O_UNSET, O_UNSET});
        }
    } else gemm_role(p, lds, b - 256);
    if (b < 256) asm volatile("s_waitcnt vmcnt(0)" ::: "memory");

}
#define ATT0_ASM \
  "s_mov_b32 s21, 8\n\t" \
  "1:\n\t" \
  "v_mfma_f32_32x32x16_bf16 v[80:95], v[128:131], v[108:111], 0\n\t" \
  "v_exp_f32 v64, v64\n\t" \
  "v_exp_f32 v65, v65\n\t" \
  "v_exp_f32 v66, v66\n\t" \
  "v_exp_f32 v67, v67\n\t" \
  "v_mfma_f32_32x32x8_bf16 v[80:95], v[140:141], v[122:123], v[80:95]\n\t" \
  "global_load_dwordx4 v[142:145], v156, s[22:23] offset:0\n\t" \
  "v_exp_f32 v68, v68\n\t" \
  "v_exp_f32 v69, v69\n\t" \
  "global_load_dwordx2 v[154:155], v157, s[22:23] offset:0\n\t" \
  "v_exp_f32 v70, v70\n\t" \
  "v_exp_f32 v71, v71\n\t" \
  "v_cvt_pk_bf16_f32 v96, v64, v65\n\t" \
  "v_cvt_pk_bf16_f32 v97, v66, v67\n\t" \
  "v_mfma_f32_32x32x16_bf16 v[48:63], v[150:153], v[100:103], v[48:63]\n\t" \
  "v_exp_f32 v72, v72\n\t" \
  "v_exp_f32 v73, v73\n\t" \
  "v_exp_f32 v74, v74\n\t" \
  "v_exp_f32 v75, v75\n\t" \
  "v_cvt_pk_bf16_f32 v98, v68, v69\n\t" \
  "v_cvt_pk_bf16_f32 v99, v70, v71\n\t" \
  "v_exp_f32 v76, v76\n\t" \
  "v_exp_f32 v77, v77\n\t" \
  "s_waitcnt vmcnt(3)\n\t" \
  "v_mfma_f32_32x32x16_bf16 v[0:15], v[132:135], v[96:99], v[0:15]\n\t" \
  "v_exp_f32 v78, v78\n\t" \
  "v_exp_f32 v79, v79\n\t" \
  "v_cvt_pk_bf16_f32 v100, v72, v73\n\t" \
  "v_cvt_pk_bf16_f32 v101, v74, v75\n\t" \
  "v_cvt_pk_bf16_f32 v102, v76, v77\n\t" \
  "v_cvt_pk_bf16_f32 v103, v78, v79\n\t" \
  "v_mfma_f32_32x32x16_bf16 v[64:79], v[128:131], v[112:115], 0\n\t" \
  "v_exp_f32 v80, v80\n\t" \
  "v_exp_f32 v81, v81\n\t" \
  "v_exp_f32 v82, v82\n\t" \
  "v_exp_f32 v83, v83\n\t" \
  "v_mfma_f32_32x32x8_bf16 v[64:79], v[140:141], v[124:125], v[64:79]\n\t" \
  "global_load_dwordx4 v[146:149], v158, s[22:23] offset:0\n\t" \
  "v_exp_f32 v84, v84\n\t" \
  "v_exp_f32 v85, v85\n\t" \
  "global_load_dwordx4 v[150:153], v158, s[22:23] offset:640\n\t" \
  "v_exp_f32 v86, v86\n\t" \
  "v_exp_f32 v87, v87\n\t" \
  "v_cvt_pk_bf16_f32 v96, v80, v81\n\t" \
  "v_cvt_pk_bf16_f32 v97, v82, v83\n\t" \
  "s_waitcnt vmcnt(4)\n\t" \
  "v_mfma_f32_32x32x16_bf16 v[0:15], v[136:139], v[100:103], v[0:15]\n\t" \
  "v_exp_f32 v88, v88\n\t" \
  "v_exp_f32 v89, v89\n\t" \
  "v_exp_f32 v90, v90\n\t" \
  "v_exp_f32 v91, v91\n\t" \
  "v_cvt_pk_bf16_f32 v98, v84, v85\n\t" \
  "v_cvt_pk_bf16_f32 v99, v86, v87\n\t" \
  "v_exp_f32 v92, v92\n\t" \
  "v_exp_f32 v93, v93\n\t" \
  "v_mfma_f32_32x32x16_bf16 v[16:31], v[132:135], v[96:99], v[16:31]\n\t" \
  "v_exp_f32 v94, v94\n\t" \
  "v_exp_f32 v95, v95\n\t" \
  "v_cvt_pk_bf16_f32 v100, v88, v89\n\t" \
  "v_cvt_pk_bf16_f32 v101, v90, v91\n\t" \
  "v_cvt_pk_bf16_f32 v102, v92, v93\n\t" \
  "v_cvt_pk_bf16_f32 v103, v94, v95\n\t" \
  "v_mfma_f32_32x32x16_bf16 v[80:95], v[128:131], v[116:119], 0\n\t" \
  "v_exp_f32 v64, v64\n\t" \
  "v_exp_f32 v65, v65\n\t" \
  "v_exp_f32 v66, v66\n\t" \
  "v_exp_f32 v67, v67\n\t" \
  "v_mfma_f32_32x32x8_bf16 v[80:95], v[140:141], v[126:127], v[80:95]\n\t" \
  "v_exp_f32 v68, v68\n\t" \
  "v_exp_f32 v69, v69\n\t" \
  "v_exp_f32 v70, v70\n\t" \
  "v_exp_f32 v71, v71\n\t" \
  "v_cvt_pk_bf16_f32 v96, v64, v65\n\t" \
  "v_cvt_pk_bf16_f32 v97, v66, v67\n\t" \
  "v_mfma_f32_32x32x16_bf16 v[16:31], v[136:139], v[100:103], v[16:31]\n\t" \
  "v_exp_f32 v72, v72\n\t" \
  "v_exp_f32 v73, v73\n\t" \
  "v_exp_f32 v74, v74\n\t" \
  "v_exp_f32 v75, v75\n\t" \
  "v_cvt_pk_bf16_f32 v98, v68, v69\n\t" \
  "v_cvt_pk_bf16_f32 v99, v70, v71\n\t" \
  "v_exp_f32 v76, v76\n\t" \
  "v_exp_f32 v77, v77\n\t" \
  "v_mfma_f32_32x32x16_bf16 v[32:47], v[132:135], v[96:99], v[32:47]\n\t" \
  "v_exp_f32 v78, v78\n\t" \
  "v_exp_f32 v79, v79\n\t" \
  "v_cvt_pk_bf16_f32 v100, v72, v73\n\t" \
  "v_cvt_pk_bf16_f32 v101, v74, v75\n\t" \
  "v_cvt_pk_bf16_f32 v102, v76, v77\n\t" \
  "v_cvt_pk_bf16_f32 v103, v78, v79\n\t" \
  "s_waitcnt vmcnt(3)\n\t" \
  "v_mfma_f32_32x32x16_bf16 v[64:79], v[142:145], v[104:107], 0\n\t" \
  "v_exp_f32 v80, v80\n\t" \
  "v_exp_f32 v81, v81\n\t" \
  "v_exp_f32 v82, v82\n\t" \
  "v_exp_f32 v83, v83\n\t" \
  "s_waitcnt vmcnt(2)\n\t" \
  "v_mfma_f32_32x32x8_bf16 v[64:79], v[154:155], v[120:121], v[64:79]\n\t" \
  "v_exp_f32 v84, v84\n\t" \
  "v_exp_f32 v85, v85\n\t" \
  "v_exp_f32 v86, v86\n\t" \
  "v_exp_f32 v87, v87\n\t" \
  "v_cvt_pk_bf16_f32 v96, v80, v81\n\t" \
  "v_cvt_pk_bf16_f32 v97, v82, v83\n\t" \
  "v_mfma_f32_32x32x16_bf16 v[32:47], v[136:139], v[100:103], v[32:47]\n\t" \
  "v_exp_f32 v88, v88\n\t" \
  "v_exp_f32 v89, v89\n\t" \
  "v_exp_f32 v90, v90\n\t" \
  "v_exp_f32 v91, v91\n\t" \
  "v_cvt_pk_bf16_f32 v98, v84, v85\n\t" \
  "v_cvt_pk_bf16_f32 v99, v86, v87\n\t" \
  "v_exp_f32 v92, v92\n\t" \
  "v_exp_f32 v93, v93\n\t" \
  "v_mfma_f32_32x32x16_bf16 v[48:63], v[132:135], v[96:99], v[48:63]\n\t" \
  "v_exp_f32 v94, v94\n\t" \
  "v_exp_f32 v95, v95\n\t" \
  "v_cvt_pk_bf16_f32 v100, v88, v89\n\t" \
  "v_cvt_pk_bf16_f32 v101, v90, v91\n\t" \
  "v_cvt_pk_bf16_f32 v102, v92, v93\n\t" \
  "v_cvt_pk_bf16_f32 v103, v94, v95\n\t" \
  "v_mfma_f32_32x32x16_bf16 v[80:95], v[142:145], v[108:111], 0\n\t" \
  "v_exp_f32 v64, v64\n\t" \
  "v_exp_f32 v65, v65\n\t" \
  "v_exp_f32 v66, v66\n\t" \
  "v_exp_f32 v67, v67\n\t" \
  "v_mfma_f32_32x32x8_bf16 v[80:95], v[154:155], v[122:123], v[80:95]\n\t" \
  "global_load_dwordx4 v[128:131], v156, s[22:23] offset:2560\n\t" \
  "v_exp_f32 v68, v68\n\t" \
  "v_exp_f32 v69, v69\n\t" \
  "global_load_dwordx2 v[140:141], v157, s[22:23] offset:2560\n\t" \
  "v_exp_f32 v70, v70\n\t" \
  "v_exp_f32 v71, v71\n\t" \
  "v_cvt_pk_bf16_f32 v96, v64, v65\n\t" \
  "v_cvt_pk_bf16_f32 v97, v66, v67\n\t" \
  "v_mfma_f32_32x32x16_bf16 v[48:63], v[136:139], v[100:103], v[48:63]\n\t" \
  "v_exp_f32 v72, v72\n\t" \
  "v_exp_f32 v73, v73\n\t" \
  "v_exp_f32 v74, v74\n\t" \
  "v_exp_f32 v75, v75\n\t" \
  "v_cvt_pk_bf16_f32 v98, v68, v69\n\t" \
  "v_cvt_pk_bf16_f32 v99, v70, v71\n\t" \
  "v_exp_f32 v76, v76\n\t" \
  "v_exp_f32 v77, v77\n\t" \
  "s_waitcnt vmcnt(3)\n\t" \
  "v_mfma_f32_32x32x16_bf16 v[0:15], v[146:149], v[96:99], v[0:15]\n\t" \
  "v_exp_f32 v78, v78\n\t" \
  "v_exp_f32 v79, v79\n\t" \
  "v_cvt_pk_bf16_f32 v100, v72, v73\n\t" \
  "v_cvt_pk_bf16_f32 v101, v74, v75\n\t" \
  "v_cvt_pk_bf16_f32 v102, v76, v77\n\t" \
  "v_cvt_pk_bf16_f32 v103, v78, v79\n\t" \
  "v_mfma_f32_32x32x16_bf16 v[64:79], v[142:145], v[112:115], 0\n\t" \
  "v_exp_f32 v80, v80\n\t" \
  "v_exp_f32 v81, v81\n\t" \
  "v_exp_f32 v82, v82\n\t" \
  "v_exp_f32 v83, v83\n\t" \
  "v_mfma_f32_32x32x8_bf16 v[64:79], v[154:155], v[124:125], v[64:79]\n\t" \
  "global_load_dwordx4 v[132:135], v158, s[22:23] offset:2560\n\t" \
  "v_exp_f32 v84, v84\n\t" \
  "v_exp_f32 v85, v85\n\t" \
  "global_load_dwordx4 v[136:139], v158, s[22:23] offset:3200\n\t" \
  "v_exp_f32 v86, v86\n\t" \
  "v_exp_f32 v87, v87\n\t" \
  "v_cvt_pk_bf16_f32 v96, v80, v81\n\t" \
  "v_cvt_pk_bf16_f32 v97, v82, v83\n\t" \
  "s_waitcnt vmcnt(4)\n\t" \
  "v_mfma_f32_32x32x16_bf16 v[0:15], v[150:153], v[100:103], v[0:15]\n\t" \
  "v_exp_f32 v88, v88\n\t" \
  "v_exp_f32 v89, v89\n\t" \
  "v_exp_f32 v90, v90\n\t" \
  "v_exp_f32 v91, v91\n\t" \
  "v_cvt_pk_bf16_f32 v98, v84, v85\n\t" \
  "v_cvt_pk_bf16_f32 v99, v86, v87\n\t" \
  "v_exp_f32 v92, v92\n\t" \
  "v_exp_f32 v93, v93\n\t" \
  "v_mfma_f32_32x32x16_bf16 v[16:31], v[146:149], v[96:99], v[16:31]\n\t" \
  "v_exp_f32 v94, v94\n\t" \
  "v_exp_f32 v95, v95\n\t" \
  "v_cvt_pk_bf16_f32 v100, v88, v89\n\t" \
  "v_cvt_pk_bf16_f32 v101, v90, v91\n\t" \
  "v_cvt_pk_bf16_f32 v102, v92, v93\n\t" \
  "v_cvt_pk_bf16_f32 v103, v94, v95\n\t" \
  "v_mfma_f32_32x32x16_bf16 v[80:95], v[142:145], v[116:119], 0\n\t" \
  "v_exp_f32 v64, v64\n\t" \
  "v_exp_f32 v65, v65\n\t" \
  "v_exp_f32 v66, v66\n\t" \
  "v_exp_f32 v67, v67\n\t" \
  "v_mfma_f32_32x32x8_bf16 v[80:95], v[154:155], v[126:127], v[80:95]\n\t" \
  "v_exp_f32 v68, v68\n\t" \
  "v_exp_f32 v69, v69\n\t" \
  "v_exp_f32 v70, v70\n\t" \
  "v_exp_f32 v71, v71\n\t" \
  "v_cvt_pk_bf16_f32 v96, v64, v65\n\t" \
  "v_cvt_pk_bf16_f32 v97, v66, v67\n\t" \
  "v_mfma_f32_32x32x16_bf16 v[16:31], v[150:153], v[100:103], v[16:31]\n\t" \
  "v_exp_f32 v72, v72\n\t" \
  "v_exp_f32 v73, v73\n\t" \
  "v_exp_f32 v74, v74\n\t" \
  "v_exp_f32 v75, v75\n\t" \
  "v_cvt_pk_bf16_f32 v98, v68, v69\n\t" \
  "v_cvt_pk_bf16_f32 v99, v70, v71\n\t" \
  "v_exp_f32 v76, v76\n\t" \
  "v_exp_f32 v77, v77\n\t" \
  "v_mfma_f32_32x32x16_bf16 v[32:47], v[146:149], v[96:99], v[32:47]\n\t" \
  "v_exp_f32 v78, v78\n\t" \
  "v_exp_f32 v79, v79\n\t" \
  "v_cvt_pk_bf16_f32 v100, v72, v73\n\t" \
  "v_cvt_pk_bf16_f32 v101, v74, v75\n\t" \
  "v_cvt_pk_bf16_f32 v102, v76, v77\n\t" \
  "v_cvt_pk_bf16_f32 v103, v78, v79\n\t" \
  "s_waitcnt vmcnt(3)\n\t" \
  "v_mfma_f32_32x32x16_bf16 v[64:79], v[128:131], v[104:107], 0\n\t" \
  "v_exp_f32 v80, v80\n\t" \
  "v_exp_f32 v81, v81\n\t" \
  "v_exp_f32 v82, v82\n\t" \
  "v_exp_f32 v83, v83\n\t" \
  "s_waitcnt vmcnt(2)\n\t" \
  "v_mfma_f32_32x32x8_bf16 v[64:79], v[140:141], v[120:121], v[64:79]\n\t" \
  "v_exp_f32 v84, v84\n\t" \
  "v_exp_f32 v85, v85\n\t" \
  "v_exp_f32 v86, v86\n\t" \
  "v_exp_f32 v87, v87\n\t" \
  "v_cvt_pk_bf16_f32 v96, v80, v81\n\t" \
  "v_cvt_pk_bf16_f32 v97, v82, v83\n\t" \
  "v_mfma_f32_32x32x16_bf16 v[32:47], v[150:153], v[100:103], v[32:47]\n\t" \
  "v_exp_f32 v88, v88\n\t" \
  "v_exp_f32 v89, v89\n\t" \
  "v_exp_f32 v90, v90\n\t" \
  "v_exp_f32 v91, v91\n\t" \
  "v_cvt_pk_bf16_f32 v98, v84, v85\n\t" \
  "v_cvt_pk_bf16_f32 v99, v86, v87\n\t" \
  "v_exp_f32 v92, v92\n\t" \
  "v_exp_f32 v93, v93\n\t" \
  "v_mfma_f32_32x32x16_bf16 v[48:63], v[146:149], v[96:99], v[48:63]\n\t" \
  "v_exp_f32 v94, v94\n\t" \
  "v_exp_f32 v95, v95\n\t" \
  "v_cvt_pk_bf16_f32 v100, v88, v89\n\t" \
  "v_cvt_pk_bf16_f32 v101, v90, v91\n\t" \
  "v_cvt_pk_bf16_f32 v102, v92, v93\n\t" \
  "v_cvt_pk_bf16_f32 v103, v94, v95\n\t" \
  "s_add_u32 s22, s22, 5120\n\t" \
  "s_addc_u32 s23, s23, 0\n\t" \
  "s_sub_u32 s21, s21, 1\n\t" \
  "s_cmp_lg_u32 s21, 0\n\t" \
  "s_cbranch_scc1 1b\n\t" \
  "s_waitcnt vmcnt(0) lgkmcnt(0)\n\t" \
  "v_mfma_f32_32x32x16_bf16 v[48:63], v[150:153], v[100:103], v[48:63]\n\t" \
  "s_nop 15\n\t" \
  ""
#define ATT2_ASM \
  "s_mov_b32 s21, 8\n\t" \
  "1:\n\t" \
  "v_mfma_f32_32x32x16_bf16 v[80:95], v[128:131], v[108:111], 0\n\t" \
  "v_exp_f32 v64, v64\n\t" \
  "v_exp_f32 v65, v65\n\t" \
  "ds_read2_b64 v[196:199], v224 offset0:28 offset1:29\n\t" \
  "v_exp_f32 v66, v66\n\t" \
  "v_mfma_f32_32x32x8_bf16 v[80:95], v[140:141], v[122:123], v[80:95]\n\t" \
  "global_load_dwordx4 v[142:145], v156, s[22:23] offset:0\n\t" \
  "v_exp_f32 v67, v67\n\t" \
  "v_exp_f32 v68, v68\n\t" \
  "ds_read_b128 v[212:215], v226 offset:3120\n\t" \
  "v_exp_f32 v69, v69\n\t" \
  "s_waitcnt lgkmcnt(4)\n\t" \
  "v_mfma_f32_32x32x16_bf16 v[160:175], v[192:195], v[208:211], v[160:175]\n\t" \
  "v_exp_f32 v70, v70\n\t" \
  "v_exp_f32 v71, v71\n\t" \
  "global_load_dwordx2 v[154:155], v157, s[22:23] offset:0\n\t" \
  "v_cvt_pk_bf16_f32 v96, v64, v65\n\t" \
  "v_cvt_pk_bf16_f32 v97, v66, v67\n\t" \
  "v_exp_f32 v72, v72\n\t" \
  "v_mfma_f32_32x32x16_bf16 v[48:63], v[150:153], v[100:103], v[48:63]\n\t" \
  "v_exp_f32 v73, v73\n\t" \
  "v_cvt_pk_bf16_f32 v98, v68, v69\n\t" \
  "v_cvt_pk_bf16_f32 v99, v70, v71\n\t" \
  "v_exp_f32 v74, v74\n\t" \
  "ds_read2_b64 v[204:207], v225 offset0:28 offset1:29\n\t" \
  "s_waitcnt vmcnt(3)\n\t" \
  "v_mfma_f32_32x32x16_bf16 v[0:15], v[132:135], v[96:99], v[0:15]\n\t" \
  "v_exp_f32 v75, v75\n\t" \
  "v_exp_f32 v76, v76\n\t" \
  "ds_read_b128 v[220:223], v227 offset:3120\n\t" \
  "v_exp_f32 v77, v77\n\t" \
  "s_waitcnt lgkmcnt(4)\n\t" \
  "v_mfma_f32_32x32x16_bf16 v[176:191], v[200:203], v[216:219], v[176:191]\n\t" \
  "v_exp_f32 v78, v78\n\t" \
  "v_exp_f32 v79, v79\n\t" \
  "v_cvt_pk_bf16_f32 v100, v72, v73\n\t" \
  "v_cvt_pk_bf16_f32 v101, v74, v75\n\t" \
  "v_cvt_pk_bf16_f32 v102, v76, v77\n\t" \
  "v_cvt_pk_bf16_f32 v103, v78, v79\n\t" \
  "v_mfma_f32_32x32x16_bf16 v[64:79], v[128:131], v[112:115], 0\n\t" \
  "v_exp_f32 v80, v80\n\t" \
  "v_exp_f32 v81, v81\n\t" \
  "ds_read2_b64 v[192:195], v224 offset0:16 offset1:17\n\t" \
  "v_exp_f32 v82, v82\n\t" \
  "v_mfma_f32_32x32x8_bf16 v[64:79], v[140:141], v[124:125], v[64:79]\n\t" \
  "global_load_dwordx4 v[146:149], v158, s[22:23] offset:0\n\t" \
  "v_exp_f32 v83, v83\n\t" \
  "v_exp_f32 v84, v84\n\t" \
  "ds_read_b128 v[208:211], v226 offset:32\n\t" \
  "v_exp_f32 v85, v85\n\t" \
  "s_waitcnt lgkmcnt(4)\n\t" \
  "v_mfma_f32_32x32x16_bf16 v[160:175], v[196:199], v[212:215], v[160:175]\n\t" \
  "v_exp_f32 v86, v86\n\t" \
  "v_exp_f32 v87, v87\n\t" \
  "global_load_dwordx4 v[150:153], v158, s[22:23] offset:640\n\t" \
  "v_cvt_pk_bf16_f32 v96, v80, v81\n\t" \
  "v_cvt_pk_bf16_f32 v97, v82, v83\n\t" \
  "v_exp_f32 v88, v88\n\t" \
  "s_waitcnt vmcnt(4)\n\t" \
  "v_mfma_f32_32x32x16_bf16 v[0:15], v[136:139], v[100:103], v[0:15]\n\t" \
  "v_exp_f32 v89, v89\n\t" \
  "v_cvt_pk_bf16_f32 v98, v84, v85\n\t" \
  "v_cvt_pk_bf16_f32 v99, v86, v87\n\t" \
  "v_exp_f32 v90, v90\n\t" \
  "ds_read2_b64 v[200:203], v225 offset0:16 offset1:17\n\t" \
  "v_mfma_f32_32x32x16_bf16 v[16:31], v[132:135], v[96:99], v[16:31]\n\t" \
  "v_exp_f32 v91, v91\n\t" \
  "v_exp_f32 v92, v92\n\t" \
  "ds_read_b128 v[216:219], v227 offset:32\n\t" \
  "v_exp_f32 v93, v93\n\t" \
  "s_waitcnt lgkmcnt(4)\n\t" \
  "v_mfma_f32_32x32x16_bf16 v[176:191], v[204:207], v[220:223], v[176:191]\n\t" \
  "v_exp_f32 v94, v94\n\t" \
  "v_exp_f32 v95, v95\n\t" \
  "v_cvt_pk_bf16_f32 v100, v88, v89\n\t" \
  "v_cvt_pk_bf16_f32 v101, v90, v91\n\t" \
  "v_cvt_pk_bf16_f32 v102, v92, v93\n\t" \
  "v_cvt_pk_bf16_f32 v103, v94, v95\n\t" \
  "v_mfma_f32_32x32x16_bf16 v[80:95], v[128:131], v[116:119], 0\n\t" \
  "v_exp_f32 v64, v64\n\t" \
  "v_exp_f32 v65, v65\n\t" \
  "ds_read2_b64 v[196:199], v224 offset0:20 offset1:21\n\t" \
  "v_exp_f32 v66, v66\n\t" \
  "v_mfma_f32_32x32x8_bf16 v[80:95], v[140:141], v[126:127], v[80:95]\n\t" \
  "v_exp_f32 v67, v67\n\t" \
  "v_exp_f32 v68, v68\n\t" \
  "ds_read_b128 v[212:215], v226 offset:3104\n\t" \
  "v_exp_f32 v69, v69\n\t" \
  "s_waitcnt lgkmcnt(4)\n\t" \
  "v_mfma_f32_32x32x16_bf16 v[160:175], v[192:195], v[208:211], v[160:175]\n\t" \
  "v_exp_f32 v70, v70\n\t" \
  "v_exp_f32 v71, v71\n\t" \
  "v_cvt_pk_bf16_f32 v96, v64, v65\n\t" \
  "v_cvt_pk_bf16_f32 v97, v66, v67\n\t" \
  "v_exp_f32 v72, v72\n\t" \
  "v_mfma_f32_32x32x16_bf16 v[16:31], v[136:139], v[100:103], v[16:31]\n\t" \
  "v_exp_f32 v73, v73\n\t" \
  "v_cvt_pk_bf16_f32 v98, v68, v69\n\t" \
  "v_cvt_pk_bf16_f32 v99, v70, v71\n\t" \
  "v_exp_f32 v74, v74\n\t" \
  "ds_read2_b64 v[204:207], v225 offset0:20 offset1:21\n\t" \
  "v_mfma_f32_32x32x16_bf16 v[32:47], v[132:135], v[96:99], v[32:47]\n\t" \
  "v_exp_f32 v75, v75\n\t" \
  "v_exp_f32 v76, v76\n\t" \
  "ds_read_b128 v[220:223], v227 offset:3104\n\t" \
  "v_exp_f32 v77, v77\n\t" \
  "s_waitcnt lgkmcnt(4)\n\t" \
  "v_mfma_f32_32x32x16_bf16 v[176:191], v[200:203], v[216:219], v[176:191]\n\t" \
  "v_exp_f32 v78, v78\n\t" \
  "v_exp_f32 v79, v79\n\t" \
  "v_cvt_pk_bf16_f32 v100, v72, v73\n\t" \
  "v_cvt_pk_bf16_f32 v101, v74, v75\n\t" \
  "v_cvt_pk_bf16_f32 v102, v76, v77\n\t" \
  "v_cvt_pk_bf16_f32 v103, v78, v79\n\t" \
  "s_waitcnt vmcnt(3)\n\t" \
  "v_mfma_f32_32x32x16_bf16 v[64:79], v[142:145], v[104:107], 0\n\t" \
  "v_exp_f32 v80, v80\n\t" \
  "v_exp_f32 v81, v81\n\t" \
  "ds_read2_b64 v[192:195], v224 offset0:8 offset1:9\n\t" \
  "v_exp_f32 v82, v82\n\t" \
  "s_waitcnt vmcnt(2)\n\t" \
  "v_mfma_f32_32x32x8_bf16 v[64:79], v[154:155], v[120:121], v[64:79]\n\t" \
  "v_exp_f32 v83, v83\n\t" \
  "v_exp_f32 v84, v84\n\t" \
  "ds_read_b128 v[208:211], v226 offset:16\n\t" \
  "v_exp_f32 v85, v85\n\t" \
  "s_waitcnt lgkmcnt(4)\n\t" \
  "v_mfma_f32_32x32x16_bf16 v[160:175], v[196:199], v[212:215], v[160:175]\n\t" \
  "v_exp_f32 v86, v86\n\t" \
  "v_exp_f32 v87, v87\n\t" \
  "v_cvt_pk_bf16_f32 v96, v80, v81\n\t" \
  "v_cvt_pk_bf16_f32 v97, v82, v83\n\t" \
  "v_exp_f32 v88, v88\n\t" \
  "v_mfma_f32_32x32x16_bf16 v[32:47], v[136:139], v[100:103], v[32:47]\n\t" \
  "v_exp_f32 v89, v89\n\t" \
  "v_cvt_pk_bf16_f32 v98, v84, v85\n\t" \
  "v_cvt_pk_bf16_f32 v99, v86, v87\n\t" \
  "v_exp_f32 v90, v90\n\t" \
  "ds_read2_b64 v[200:203], v225 offset0:8 offset1:9\n\t" \
  "v_mfma_f32_32x32x16_bf16 v[48:63], v[132:135], v[96:99], v[48:63]\n\t" \
  "v_exp_f32 v91, v91\n\t" \
  "v_exp_f32 v92, v92\n\t" \
  "ds_read_b128 v[216:219], v227 offset:16\n\t" \
  "v_exp_f32 v93, v93\n\t" \
  "s_waitcnt lgkmcnt(4)\n\t" \
  "v_mfma_f32_32x32x16_bf16 v[176:191], v[204:207], v[220:223], v[176:191]\n\t" \
  "v_exp_f32 v94, v94\n\t" \
  "v_exp_f32 v95, v95\n\t" \
  "v_cvt_pk_bf16_f32 v100, v88, v89\n\t" \
  "v_cvt_pk_bf16_f32 v101, v90, v91\n\t" \
  "v_cvt_pk_bf16_f32 v102, v92, v93\n\t" \
  "v_cvt_pk_bf16_f32 v103, v94, v95\n\t" \
  "v_mfma_f32_32x32x16_bf16 v[80:95], v[142:145], v[108:111], 0\n\t" \
  "v_exp_f32 v64, v64\n\t" \
  "v_exp_f32 v65, v65\n\t" \
  "ds_read2_b64 v[196:199], v224 offset0:12 offset1:13\n\t" \
  "v_exp_f32 v66, v66\n\t" \
  "v_mfma_f32_32x32x8_bf16 v[80:95], v[154:155], v[122:123], v[80:95]\n\t" \
  "global_load_dwordx4 v[128:131], v156, s[22:23] offset:2560\n\t" \
  "v_exp_f32 v67, v67\n\t" \
  "v_exp_f32 v68, v68\n\t" \
  "ds_read_b128 v[212:215], v226 offset:3088\n\t" \
  "v_exp_f32 v69, v69\n\t" \
  "s_waitcnt lgkmcnt(4)\n\t" \
  "v_mfma_f32_32x32x16_bf16 v[160:175], v[192:195], v[208:211], v[160:175]\n\t" \
  "v_exp_f32 v70, v70\n\t" \
  "v_exp_f32 v71, v71\n\t" \
  "global_load_dwordx2 v[140:141], v157, s[22:23] offset:2560\n\t" \
  "v_cvt_pk_bf16_f32 v96, v64, v65\n\t" \
  "v_cvt_pk_bf16_f32 v97, v66, v67\n\t" \
  "v_exp_f32 v72, v72\n\t" \
  "v_mfma_f32_32x32x16_bf16 v[48:63], v[136:139], v[100:103], v[48:63]\n\t" \
  "v_exp_f32 v73, v73\n\t" \
  "v_cvt_pk_bf16_f32 v98, v68, v69\n\t" \
  "v_cvt_pk_bf16_f32 v99, v70, v71\n\t" \
  "v_exp_f32 v74, v74\n\t" \
  "ds_read2_b64 v[204:207], v225 offset0:12 offset1:13\n\t" \
  "s_waitcnt vmcnt(3)\n\t" \
  "v_mfma_f32_32x32x16_bf16 v[0:15], v[146:149], v[96:99], v[0:15]\n\t" \
  "v_exp_f32 v75, v75\n\t" \
  "v_exp_f32 v76, v76\n\t" \
  "ds_read_b128 v[220:223], v227 offset:3088\n\t" \
  "v_exp_f32 v77, v77\n\t" \
  "s_waitcnt lgkmcnt(4)\n\t" \
  "v_mfma_f32_32x32x16_bf16 v[176:191], v[200:203], v[216:219], v[176:191]\n\t" \
  "v_exp_f32 v78, v78\n\t" \
  "v_exp_f32 v79, v79\n\t" \
  "v_cvt_pk_bf16_f32 v100, v72, v73\n\t" \
  "v_cvt_pk_bf16_f32 v101, v74, v75\n\t" \
  "v_cvt_pk_bf16_f32 v102, v76, v77\n\t" \
  "v_cvt_pk_bf16_f32 v103, v78, v79\n\t" \
  "v_mfma_f32_32x32x16_bf16 v[64:79], v[142:145], v[112:115], 0\n\t" \
  "v_exp_f32 v80, v80\n\t" \
  "v_exp_f32 v81, v81\n\t" \
  "ds_read2_b64 v[192:195], v224 offset0:0 offset1:1\n\t" \
  "v_exp_f32 v82, v82\n\t" \
  "v_mfma_f32_32x32x8_bf16 v[64:79], v[154:155], v[124:125], v[64:79]\n\t" \
  "global_load_dwordx4 v[132:135], v158, s[22:23] offset:2560\n\t" \
  "v_exp_f32 v83, v83\n\t" \
  "v_exp_f32 v84, v84\n\t" \
  "ds_read_b128 v[208:211], v226 offset:0\n\t" \
  "v_exp_f32 v85, v85\n\t" \
  "s_waitcnt lgkmcnt(4)\n\t" \
  "v_mfma_f32_32x32x16_bf16 v[160:175], v[196:199], v[212:215], v[160:175]\n\t" \
  "v_exp_f32 v86, v86\n\t" \
  "v_exp_f32 v87, v87\n\t" \
  "global_load_dwordx4 v[136:139], v158, s[22:23] offset:3200\n\t" \
  "v_cvt_pk_bf16_f32 v96, v80, v81\n\t" \
  "v_cvt_pk_bf16_f32 v97, v82, v83\n\t" \
  "v_exp_f32 v88, v88\n\t" \
  "s_waitcnt vmcnt(4)\n\t" \
  "v_mfma_f32_32x32x16_bf16 v[0:15], v[150:153], v[100:103], v[0:15]\n\t" \
  "v_exp_f32 v89, v89\n\t" \
  "v_cvt_pk_bf16_f32 v98, v84, v85\n\t" \
  "v_cvt_pk_bf16_f32 v99, v86, v87\n\t" \
  "v_exp_f32 v90, v90\n\t" \
  "ds_read2_b64 v[200:203], v225 offset0:0 offset1:1\n\t" \
  "v_mfma_f32_32x32x16_bf16 v[16:31], v[146:149], v[96:99], v[16:31]\n\t" \
  "v_exp_f32 v91, v91\n\t" \
  "v_exp_f32 v92, v92\n\t" \
  "ds_read_b128 v[216:219], v227 offset:0\n\t" \
  "v_exp_f32 v93, v93\n\t" \
  "s_waitcnt lgkmcnt(4)\n\t" \
  "v_mfma_f32_32x32x16_bf16 v[176:191], v[204:207], v[220:223], v[176:191]\n\t" \
  "v_exp_f32 v94, v94\n\t" \
  "v_exp_f32 v95, v95\n\t" \
  "v_cvt_pk_bf16_f32 v100, v88, v89\n\t" \
  "v_cvt_pk_bf16_f32 v101, v90, v91\n\t" \
  "v_cvt_pk_bf16_f32 v102, v92, v93\n\t" \
  "v_cvt_pk_bf16_f32 v103, v94, v95\n\t" \
  "v_mfma_f32_32x32x16_bf16 v[80:95], v[142:145], v[116:119], 0\n\t" \
  "v_exp_f32 v64, v64\n\t" \
  "v_exp_f32 v65, v65\n\t" \
  "ds_read2_b64 v[196:199], v224 offset0:4 offset1:5\n\t" \
  "v_exp_f32 v66, v66\n\t" \
  "v_mfma_f32_32x32x8_bf16 v[80:95], v[154:155], v[126:127], v[80:95]\n\t" \
  "v_exp_f32 v67, v67\n\t" \
  "v_exp_f32 v68, v68\n\t" \
  "ds_read_b128 v[212:215], v226 offset:3072\n\t" \
  "v_exp_f32 v69, v69\n\t" \
  "s_waitcnt lgkmcnt(4)\n\t" \
  "v_mfma_f32_32x32x16_bf16 v[160:175], v[192:195], v[208:211], v[160:175]\n\t" \
  "v_exp_f32 v70, v70\n\t" \
  "v_exp_f32 v71, v71\n\t" \
  "v_cvt_pk_bf16_f32 v96, v64, v65\n\t" \
  "v_cvt_pk_bf16_f32 v97, v66, v67\n\t" \
  "v_exp_f32 v72, v72\n\t" \
  "v_mfma_f32_32x32x16_bf16 v[16:31], v[150:153], v[100:103], v[16:31]\n\t" \
  "v_exp_f32 v73, v73\n\t" \
  "v_cvt_pk_bf16_f32 v98, v68, v69\n\t" \
  "v_cvt_pk_bf16_f32 v99, v70, v71\n\t" \
  "v_exp_f32 v74, v74\n\t" \
  "ds_read2_b64 v[204:207], v225 offset0:4 offset1:5\n\t" \
  "v_mfma_f32_32x32x16_bf16 v[32:47], v[146:149], v[96:99], v[32:47]\n\t" \
  "v_exp_f32 v75, v75\n\t" \
  "v_exp_f32 v76, v76\n\t" \
  "ds_read_b128 v[220:223], v227 offset:3072\n\t" \
  "v_exp_f32 v77, v77\n\t" \
  "s_waitcnt lgkmcnt(4)\n\t" \
  "v_mfma_f32_32x32x16_bf16 v[176:191], v[200:203], v[216:219], v[176:191]\n\t" \
  "v_exp_f32 v78, v78\n\t" \
  "v_exp_f32 v79, v79\n\t" \
  "v_cvt_pk_bf16_f32 v100, v72, v73\n\t" \
  "v_cvt_pk_bf16_f32 v101, v74, v75\n\t" \
  "v_cvt_pk_bf16_f32 v102, v76, v77\n\t" \
  "v_cvt_pk_bf16_f32 v103, v78, v79\n\t" \
  "s_waitcnt vmcnt(3)\n\t" \
  "v_mfma_f32_32x32x16_bf16 v[64:79], v[128:131], v[104:107], 0\n\t" \
  "v_exp_f32 v80, v80\n\t" \
  "v_exp_f32 v81, v81\n\t" \
  "v_subrev_u32 v224, 256, v224\n\t" \
  "ds_read2_b64 v[192:195], v224 offset0:24 offset1:25\n\t" \
  "v_exp_f32 v82, v82\n\t" \
  "s_waitcnt vmcnt(2)\n\t" \
  "v_mfma_f32_32x32x8_bf16 v[64:79], v[140:141], v[120:121], v[64:79]\n\t" \
  "v_exp_f32 v83, v83\n\t" \
  "v_exp_f32 v84, v84\n\t" \
  "v_subrev_u32 v226, 64, v226\n\t" \
  "ds_read_b128 v[208:211], v226 offset:48\n\t" \
  "v_exp_f32 v85, v85\n\t" \
  "s_waitcnt lgkmcnt(4)\n\t" \
  "v_mfma_f32_32x32x16_bf16 v[160:175], v[196:199], v[212:215], v[160:175]\n\t" \
  "v_exp_f32 v86, v86\n\t" \
  "v_exp_f32 v87, v87\n\t" \
  "v_cvt_pk_bf16_f32 v96, v80, v81\n\t" \
  "v_cvt_pk_bf16_f32 v97, v82, v83\n\t" \
  "v_exp_f32 v88, v88\n\t" \
  "v_mfma_f32_32x32x16_bf16 v[32:47], v[150:153], v[100:103], v[32:47]\n\t" \
  "v_exp_f32 v89, v89\n\t" \
  "v_cvt_pk_bf16_f32 v98, v84, v85\n\t" \
  "v_cvt_pk_bf16_f32 v99, v86, v87\n\t" \
  "v_exp_f32 v90, v90\n\t" \
  "v_subrev_u32 v225, 256, v225\n\t" \
  "ds_read2_b64 v[200:203], v225 offset0:24 offset1:25\n\t" \
  "v_mfma_f32_32x32x16_bf16 v[48:63], v[146:149], v[96:99], v[48:63]\n\t" \
  "v_exp_f32 v91, v91\n\t" \
  "v_exp_f32 v92, v92\n\t" \
  "v_subrev_u32 v227, 64, v227\n\t" \
  "ds_read_b128 v[216:219], v227 offset:48\n\t" \
  "v_exp_f32 v93, v93\n\t" \
  "s_waitcnt lgkmcnt(4)\n\t" \
  "v_mfma_f32_32x32x16_bf16 v[176:191], v[204:207], v[220:223], v[176:191]\n\t" \
  "v_exp_f32 v94, v94\n\t" \
  "v_exp_f32 v95, v95\n\t" \
  "v_cvt_pk_bf16_f32 v100, v88, v89\n\t" \
  "v_cvt_pk_bf16_f32 v101, v90, v91\n\t" \
  "v_cvt_pk_bf16_f32 v102, v92, v93\n\t" \
  "v_cvt_pk_bf16_f32 v103, v94, v95\n\t" \
  "s_add_u32 s22, s22, 5120\n\t" \
  "s_addc_u32 s23, s23, 0\n\t" \
  "s_sub_u32 s21, s21, 1\n\t" \
  "s_cmp_lg_u32 s21, 0\n\t" \
  "s_cbranch_scc1 1b\n\t" \
  "s_waitcnt vmcnt(0) lgkmcnt(0)\n\t" \
  "v_mfma_f32_32x32x16_bf16 v[48:63], v[150:153], v[100:103], v[48:63]\n\t" \
  "s_nop 15\n\t" \
  ""
#define R_O0 "{v[0:15]}"
#define R_QM0 "{v[104:107]}"
#define R_QE0 "{v[120:121]}"
#define R_O1 "{v[16:31]}"
#define R_QM1 "{v[108:111]}"
#define R_QE1 "{v[122:123]}"
#define R_O2 "{v[32:47]}"
#define R_QM2 "{v[112:115]}"
#define R_QE2 "{v[124:125]}"
#define R_O3 "{v[48:63]}"
#define R_QM3 "{v[116:119]}"
#define R_QE3 "{v[126:127]}"
#define R_SA "{v[64:79]}"
#define R_SB "{v[80:95]}"
#define R_P0 "{v[96:99]}"
#define R_P1 "{v[100:103]}"
#define R_KM0 "{v[128:131]}"
#define R_KE0 "{v[140:141]}"
#define R_V00 "{v[132:135]}"
#define R_V10 "{v[136:139]}"
#define R_KM1 "{v[142:145]}"
#define R_KE1 "{v[154:155]}"
#define R_V01 "{v[146:149]}"
#define R_V11 "{v[150:153]}"
#define R_OKM "{v156}"
#define R_OKE "{v157}"
#define R_OV "{v158}"
#define R_Y0 "{v[160:175]}"
#define R_OF0 "{v224}"
#define R_OG0 "{v226}"
#define R_F00 "{v[192:195]}"
#define R_G00 "{v[208:211]}"
#define R_F01 "{v[196:199]}"
#define R_G01 "{v[212:215]}"
#define R_Y1 "{v[176:191]}"
#define R_OF1 "{v225}"
#define R_OG1 "{v227}"
#define R_F10 "{v[200:203]}"
#define R_G10 "{v[216:219]}"
#define R_F11 "{v[204:207]}"
#define R_G11 "{v[220:223]}"
constexpr int A_FC = 2208, A_FRNG = 4 * A_FC;
constexpr int A_GIMG = 6144;
constexpr int A_FR = 128, A_GI = A_FR + 6 * A_FRNG, A_SLOT = 2560;
constexpr int T_WO = 128;
constexpr int A_XO = 83200, T_Y = A_XO + 12 * A_SLOT, T_RED = T_Y + 72 * 144, T_X = T_RED + 8 * 3072, T_G = T_X + 64 * 144, T_D = T_G + 256, K2_LDS = T_D + 160 + 256;
static_assert(A_GI + 4 * A_GIMG <= A_XO && T_WO + 576 * 144 + 16 <= A_XO, "LDS map");
constexpr unsigned SPIN_CAP = 20000;
constexpr float SAFE_BOUND = 40.0f;
#define MFMA16(a, b, c) __builtin_amdgcn_mfma_f32_32x32x16_bf16(a, b, c, 0, 0, 0)
#define MFMA8(a, b, c) __builtin_amdgcn_mfma_f32_32x32x8bf16_1k(a, b, c, 0, 0, 0)
typedef __attribute__((address_space(4))) const unsigned* cptr32;

DI void lds_arrive(LAS unsigned* c, int lane) {
    asm volatile("s_waitcnt vmcnt(0) lgkmcnt(0)" ::: "memory");
    if (lane == 0) __hip_atomic_fetch_add(c, 1u, __ATOMIC_RELAXED, __HIP_MEMORY_SCOPE_WORKGROUP);
}
DI void lds_wait(LAS unsigned* c, unsigned n) {
    while ((unsigned)rfl((int)__hip_atomic_load(c, __ATOMIC_RELAXED, __HIP_MEMORY_SCOPE_WORKGROUP)) < n) __builtin_amdgcn_s_sleep(1);
    asm volatile("" ::: "memory");
}

typedef __attribute__((address_space(1))) unsigned gu32;

#define LD_SC1(dst, ptr, off) asm volatile("global_load_dwordx4 %0, %1, off offset:" #off " sc1" : "=v"(dst) : "v"(ptr) : "memory")
template <int N> DI void wait_vm(u32x4& v) { asm volatile("s_waitcnt vmcnt(%1)" : "+v"(v) : "n"(N)); }
template <int KH>
DI void out_tail(const Params& p, LAS char* lds, int rbk, int jb, int tt, int lane, const bf16x8 (&aw)[6][5]) {
    constexpr int M0 = KH ? 3 : 0, NM = KH ? 2 : 3, O0 = KH ? 0 : 3, NO = KH ? 3 : 2;
    const int fr = lane & 15, g = lane >> 4, tid = TIDX;
    const char* of = p.ws + WS_OF + (size_t)((rbk * 4 + tt) * 18 + 9 * KH) * 1024 + lane * 16;
    const char* ys = p.ws + WS_YT + (size_t)(72 * jb) * 4096 + rbk * 128;
    const int y1 = min(tid + 512, 575);
    const char *of1 = of + 4096, *of2 = of + 8192, *ya = ys + (size_t)(tid >> 3) * 4096 + (tid & 7) * 16, *yb = ys + (size_t)(y1 >> 3) * 4096 + (y1 & 7) * 16;
    u32x4 bo[9], yv0, yv1;
    LD_SC1(yv0, ya, 0); LD_SC1(yv1, yb, 0);
    LAS const char* wt = lds + T_WO + (9 * KH * 32 + 16 * (g >> 1) + 4 * (g & 1) + (fr >> 2)) * 144 + (lane & 3) * 8;
    auto rdA = [&](bf16x8 (&a)[5], int ks) {
#pragma unroll
        for (int m = 0; m < 5; ++m) { LAS const char* s = wt + ks * 32 * 144 + m * 32; a[m] = cat8(trread(s), trread(s + 8 * 144)); }
    };
    f32x4 acc[5];
    for (unsigned tries = 0;; ++tries) {
        LD_SC1(bo[0], of, 0); LD_SC1(bo[1], of, 1024); LD_SC1(bo[2], of, 2048); LD_SC1(bo[3], of, 3072);
        LD_SC1(bo[4], of1, 0); LD_SC1(bo[5], of1, 1024); LD_SC1(bo[6], of1, 2048); LD_SC1(bo[7], of1, 3072);
        LD_SC1(bo[8], of2, 0);
#pragma unroll
        for (int m = 0; m < 5; ++m) acc[m] = f32x4{};
        unsigned top = 0;
        bf16x8 a6[5], a7[5], a8[5];
        rdA(a6, 6);
        wait_vm<8>(bo[0]);
#define KSTEP(ks, A) wait_vm<8 - ks>(bo[ks]); __builtin_amdgcn_sched_barrier(0); \
        _Pragma("unroll") for (int m = 0; m < 5; ++m) acc[m] = __builtin_amdgcn_mfma_f32_16x16x32_bf16(A[m], __builtin_bit_cast(bf16x8, bo[ks]), acc[m], 0, 0, 0); \
        top = max(max(top, bo[ks][0]), max(max(bo[ks][1], bo[ks][2]), bo[ks][3]));
        KSTEP(0, aw[0]) KSTEP(1, aw[1]) KSTEP(2, aw[2]) rdA(a7, 7); KSTEP(3, aw[3]) KSTEP(4, aw[4]) KSTEP(5, aw[5]) KSTEP(6, a6) rdA(a8, 8); KSTEP(7, a7) KSTEP(8, a8)
#undef KSTEP
        if (__builtin_expect(!__any(top == O_UNSET), 1) || tries >= SPIN_CAP) break;
        __builtin_amdgcn_s_sleep(2);
    }

    asm volatile("s_waitcnt vmcnt(0)" : "+v"(yv0), "+v"(yv1));
    __builtin_amdgcn_sched_barrier(0);
    *(LAS u32x4*)(lds + T_Y + (tid >> 3) * 144 + (tid & 7) * 16) = yv0;
    if (tid < 64) *(LAS u32x4*)(lds + T_Y + (y1 >> 3) * 144 + (y1 & 7) * 16) = yv1;
    LAS char* red = lds + T_RED + (tt * 2) * 3072;
#pragma unroll
    for (int m = 0; m < NO; ++m) *(LAS f32x4*)(red + KH * 3072 + m * 1024 + lane * 16) = acc[O0 + m];
    const int tl = tt * 16 + fr;
    const float gate = *(LAS const float*)(lds + T_G + tl * 4), omg = 1.0f - gate;
    u32x2 xu[NM], dk[NM];
#pragma unroll
    for (int m = 0; m < NM; ++m) {
        const int dc = min(16 * (M0 + m) + 4 * g, 68);
        xu[m] = *(LAS const u32x2*)(lds + T_X + tl * 144 + dc * 2);
        dk[m] = *(LAS const u32x2*)(lds + T_D + dc * 2);
    }
    asm volatile("s_waitcnt lgkmcnt(0)\n\ts_barrier" ::: "memory");

#pragma unroll
    for (int m = 0; m < NM; ++m) {
        const f32x4 o = *(LAS const f32x4*)(red + (1 - KH) * 3072 + m * 1024 + lane * 16);
        const int dl = 16 * (M0 + m) + 4 * g, dc = min(dl, 68);
        float r[4];
#pragma unroll
        for (int e = 0; e < 4; ++e) {
            const float y = bf2f(*(LAS const unsigned short*)(lds + T_Y + (dc + e) * 144 + tl * 2));
            const float u = (e & 1) ? bfhi(xu[m][e >> 1]) : bflo(xu[m][e >> 1]), dd = (e & 1) ? bfhi(dk[m][e >> 1]) : bflo(dk[m][e >> 1]);
            r[e] = __builtin_fmaf(gate, __builtin_fmaf(u, dd, y), omg * (acc[M0 + m][e] + o[e]));
        }
        if (dl < 72) *(u32x2*)(p.out + (size_t)(rbk * 64 + tl) * DM + 72 * jb + dl) = u32x2{pkbf(r[0], r[1]), pkbf(r[2], r[3])};
    }
}

__global__ __launch_bounds__(512, 2) void k2_attn(Params p) {
    extern __shared__ __attribute__((aligned(16))) char smem[];
    LAS char* lds = (LAS char*)smem;
    LAS unsigned* sync = (LAS unsigned*)lds;
    const int lane = TIDX & 63, wave = rfl(TIDX >> 6), b = BIDX;
    const int head = (b & 7) * 4 + ((b >> 3) & 3), qb = b >> 5, qg = wave >> 2, kq = wave & 3;
    const int r = lane & 31, hh = lane >> 5;
    const unsigned long long entry = code_entry();

    if (qg == 1) {
        code_prefetch(entry, kq, (unsigned)(size_t)lds + T_D + 160, lane);
        if (kq == 0 && lane < 24) code_prefetch(entry, 4, (unsigned)(size_t)lds + T_D + 160, lane);
    }
    if (qg == 1) __builtin_amdgcn_s_setprio(1);
    unsigned kmu = 0;
    {
        cptr32 kp = (cptr32)(p.ws + WS_KMAX) + head * 32;
#pragma unroll
        for (int i = 0; i < 32; ++i) kmu = max(kmu, kp[i]);
    }
    bf16x8 qm[4]; u32x2 qe[4];
    {
        const char* qmb = p.ws + WS_QM + (size_t)(head * 64 + qb * 8 + qg * 4) * 1024;
        const char* qeb = p.ws + WS_QE + (size_t)(head * 64 + qb * 8 + qg * 4) * 256;
#pragma unroll
        for (int i = 0; i < 4; ++i) {
            const int qt = (kq + i) & 3;
            qm[i] = *(const bf16x8*)(qmb + qt * 1024 + lane * 16); qe[i] = *(const u32x2*)(qeb + qt * 256 + r * 8);
        }
    }
    const char* kvb = p.ws + WS_KV + (size_t)(head * 64 + kq * 16) * KV_TILE;
    const int offKm = lane * 16, offKe = hh ? 1280 + 19 * 16 : 1024 + r * 8, offV = 1280 + hh * 320 + min(r, 19) * 16;
    bf16x8 km0 = *(const bf16x8*)(kvb + offKm), v00 = *(const bf16x8*)(kvb + offV), v10 = *(const bf16x8*)(kvb + offV + 640);
    bf16x4 ke0 = *(const bf16x4*)(kvb + offKe);
    if (TIDX < 8) sync[TIDX] = 0u;
    asm volatile("s_waitcnt lgkmcnt(0)\n\ts_barrier" ::: "memory");
    const int oct = b >> 3, w8 = (b + oct) & 7, cha = 18 * oct + 2 * w8, chc = 18 * oct + 16;
    const int spare = w8 < 2 ? 1 : (w8 == 2 ? 2 : 0);
    const bool conv = qg == 1 && (kq < 3 || spare != 0), unitB = kq < 2 || (kq == 3 && spare == 1);
    const unsigned ldsb = (unsigned)(size_t)lds;
    const int rbk = 4 * (b & 7) + ((b >> 3) & 3), jb = b >> 5;
    if (__builtin_expect(qg == 1, 0)) {
        asm volatile("s_waitcnt vmcnt(0)" ::: "memory");
        const unsigned v16 = lane * 16, v64 = lane * 64;
        const char* krb = p.ws + WS_KR + 2 * kq;
        const char* utb = p.ws + WS_UT + kq * 16;
        const int ti = min(lane >> 2, 5), tch = ti < 4 ? cha + (ti >> 1) : (spare == 1 ? chc + w8 : chc + (ti & 1));
        const int thi = (ti < 4 || spare == 1) ? (ti & 1) : 0;
        const u32x4 tail = *(const u32x4*)(krb + (size_t)tch * KR_ROW + (1 - thi) * 2048 + 2048 + (lane & 3) * 16);
#pragma unroll
        for (int i = 0; i < 6; ++i) {
            if (i < 4 || spare) {
                const int chn = i < 4 ? cha + (i >> 1) : (spare == 1 ? chc + w8 : chc + (i & 1));
                const int hi = (i < 4 || spare == 1) ? (i & 1) : 0;
                const char* fs = krb + (size_t)chn * KR_ROW + (1 - hi) * 2048;
                const unsigned fd = ldsb + A_FR + i * A_FRNG + kq * A_FC;
                dma16(v16, fs, fd); dma16(v16, fs + 1024, fd + 1024);
            }
        }
#pragma unroll
        for (int i = 0; i < 4; ++i) {
            if (i < 2 || (i == 2 && spare) || spare == 2) {
                const int chn = i < 2 ? cha + i : (i == 2 && spare == 1 ? chc + w8 : chc + (i - 2));
                dma16(v64, utb + (size_t)chn * 4096, ldsb + A_GI + i * A_GIMG + kq * 1536 + 512);
                if (lane < 32) *(LAS u32x4*)(lds + A_GI + i * A_GIMG + kq * 1536 + v16) = u32x4{0u, 0u, 0u, 0u};
            }
        }
        if (lane < 24) *(LAS u32x4*)(lds + A_FR + ti * A_FRNG + kq * A_FC + 2048 + (lane & 3) * 16) = tail;
    }
    const float kmx = __uint_as_float(kmu);
    float mq[4]; bool unsafe = false;
#pragma unroll
    for (int i = 0; i < 4; ++i) { mq[i] = __uint_as_float(qe[i][1]) * kmx; unsafe |= !(mq[i] <= SAFE_BOUND); }
    const f32x16 zero = {};
    if (__builtin_expect(__any(unsafe), 0)) {
#pragma unroll
        for (int i = 0; i < 4; ++i) {
            const bf16x4 qx = __builtin_bit_cast(bf16x4, u32x2{hh ? 0u : qe[i][0], 0u});
            float mx = -3.0e38f;
            for (int kt = 0; kt < 16; ++kt) {
                const bf16x8 a = *(const bf16x8*)(kvb + kt * KV_TILE + offKm);
                const bf16x4 e = *(const bf16x4*)(kvb + kt * KV_TILE + offKe);
                f32x16 s = MFMA16(a, qm[i], zero); s = MFMA8(e, qx, s);
#pragma unroll
                for (int k = 0; k < 16; ++k) mx = fmaxf(mx, s[k]);
            }
            mq[i] = fmaxf(mx, __shfl_xor(mx, 32));
        }
    }
    bf16x4 qx[4];
#pragma unroll
    for (int i = 0; i < 4; ++i) {
        const unsigned hi = pkbf(-mq[i], 0.f) & 0xffffu;
        const unsigned sh = hi | (pkbf(-mq[i] - bflo(hi), 0.f) << 16);
        mq[i] = -(bflo(sh) + bfhi(sh));
        qx[i] = __builtin_bit_cast(bf16x4, hh ? u32x2{0u, 0u} : u32x2{qe[i][0], sh});
    }

    f32x16 O0 = {}, O1 = {}, O2 = {}, O3 = {}, Y0 = {}, Y1 = {};
    f32x16 sA = MFMA16(km0, qm[0], zero); sA = MFMA8(ke0, qx[0], sA);
    f32x16 sB; u32x4 p0, p1 = {0u, 0u, 0u, 0u};
    bf16x8 km1, v01, v11 = v00; bf16x4 ke1;
    const unsigned long long kva = (unsigned long long)(kvb + KV_TILE);
    const unsigned long long skv = ((unsigned long long)(unsigned)rfl((int)(kva >> 32)) << 32) | (unsigned)rfl((int)kva);
#define ATT_OUT "+" R_O0(O0), "+" R_O1(O1), "+" R_O2(O2), "+" R_O3(O3), "+" R_SA(sA), "=" R_SB(sB), "=" R_P0(p0), "+" R_P1(p1), \
              "+" R_KM0(km0), "+" R_KE0(ke0), "+" R_V00(v00), "+" R_V10(v10), "=" R_KM1(km1), "=" R_KE1(ke1), "=" R_V01(v01), "+" R_V11(v11)
#define ATT_IN R_QM0(qm[0]), R_QM1(qm[1]), R_QM2(qm[2]), R_QM3(qm[3]), R_QE0(qx[0]), R_QE1(qx[1]), R_QE2(qx[2]), R_QE3(qx[3]), \
              R_OKM(offKm), R_OKE(offKe), R_OV(offV), [skv] "s"(skv)
    if (qg == 1) lds_arrive(sync + 0, lane);
    if (__builtin_expect(conv, 0)) {
        lds_wait(sync + 0, 4);
        const int f0i = kq == 0 ? 0 : (kq == 1 ? 2 : (kq == 2 ? 0 : 4)), f1i = kq == 0 ? 1 : (kq == 1 ? 3 : (kq == 2 ? 2 : 5));
        const int g0i = kq < 2 ? kq : (kq == 2 ? 0 : 2), g1i = kq < 2 ? kq : (kq == 2 ? 1 : (spare == 1 ? 2 : 3));
        const int x0 = 1023 - r + 8 * hh, fc = x0 & 3, fl = A_FR + fc * A_FC + 2 * (x0 - fc), gl = A_GI + hh * 1536 + (32 + r) * 16;
        const int fa0 = fl + f0i * A_FRNG, fa1 = fl + f1i * A_FRNG, ga0 = gl + g0i * A_GIMG + (unitB ? 512 : 0), ga1 = gl + g1i * A_GIMG;
        int oF0 = (int)ldsb + fa0 - 192, oF1 = (int)ldsb + fa1 - 192, oG0 = (int)ldsb + ga0 - 48, oG1 = (int)ldsb + ga1 - 48;
        bf16x8 f00 = cat8(*(LAS const bf16x4*)(lds + fa0), *(LAS const bf16x4*)(lds + fa0 + 8)), f01;
        bf16x8 f10 = cat8(*(LAS const bf16x4*)(lds + fa1), *(LAS const bf16x4*)(lds + fa1 + 8)), f11;
        bf16x8 g00 = *(LAS const bf16x8*)(lds + ga0), g01, g10 = *(LAS const bf16x8*)(lds + ga1), g11;

        asm volatile("s_nop 15\n\ts_mov_b64 s[22:23], %[skv]\n\t" ATT2_ASM
            : ATT_OUT, "+" R_Y0(Y0), "+" R_Y1(Y1), "+" R_F00(f00), "=" R_F01(f01), "+" R_F10(f10), "=" R_F11(f11),
              "+" R_G00(g00), "=" R_G01(g01), "+" R_G10(g10), "=" R_G11(g11), "+" R_OF0(oF0), "+" R_OF1(oF1), "+" R_OG0(oG0), "+" R_OG1(oG1)
            : ATT_IN : "s21", "s22", "s23", "scc", "memory");
        char* y0 = p.ws + WS_YT + (32 * r + 4 * hh) * 2;
        if (unitB) {
            char* yt = y0 + (size_t)(kq < 2 ? cha + kq : chc + w8) * 4096 + 2048;
#pragma unroll
            for (int k = 0; k < 4; ++k)
                st8(yt + k * 16, u32x2{pkbf(Y0[4 * k] + Y1[4 * k], Y0[4 * k + 1] + Y1[4 * k + 1]), pkbf(Y0[4 * k + 2] + Y1[4 * k + 2], Y0[4 * k + 3] + Y1[4 * k + 3])});
        } else {
            char* yt = y0 + (size_t)(kq == 2 ? cha : chc) * 4096;
#pragma unroll
            for (int k = 0; k < 4; ++k) {
                st8(yt + k * 16, u32x2{pkbf(Y0[4 * k], Y0[4 * k + 1]), pkbf(Y0[4 * k + 2], Y0[4 * k + 3])});
                st8(yt + 4096 + k * 16, u32x2{pkbf(Y1[4 * k], Y1[4 * k + 1]), pkbf(Y1[4 * k + 2], Y1[4 * k + 3])});
            }
        }
    } else {

        asm volatile("s_nop 15\n\ts_mov_b64 s[22:23], %[skv]\n\t" ATT0_ASM : ATT_OUT : ATT_IN : "s21", "s22", "s23", "scc", "memory");
    }

    auto putO = [&](const f32x16& O, int i) {
        LAS char* s = lds + A_XO + (((kq + i) & 3) * 3 + 3 - i) * A_SLOT;
        *(LAS f32x4*)(s + lane * 16) = f32x4{O[0], O[1], O[2], O[3]};
        *(LAS f32x4*)(s + 1024 + lane * 16) = f32x4{O[4], O[5], O[6], O[7]};
        if (hh == 0) *(LAS f32x4*)(s + 2048 + r * 16) = f32x4{O[8], O[9], O[10], mq[i]};
    };
    if (qg == 0) lds_wait(sync + 2, 4);
    putO(O1, 1); putO(O2, 2); putO(O3, 3);
    asm volatile("s_waitcnt lgkmcnt(0)" ::: "memory");
    if (lane == 0) __hip_atomic_fetch_add(sync + (qg ? 3 : 1), 1u, __ATOMIC_RELAXED, __HIP_MEMORY_SCOPE_WORKGROUP);
    const float o16 = __uint_as_float(__builtin_amdgcn_permlane32_swap(__float_as_uint(O0[8]), __float_as_uint(O0[8]), false, false)[0]);
    const float o17 = __uint_as_float(__builtin_amdgcn_permlane32_swap(__float_as_uint(O0[9]), __float_as_uint(O0[9]), false, false)[0]);
    const float ol = __uint_as_float(__builtin_amdgcn_permlane32_swap(__float_as_uint(O0[10]), __float_as_uint(O0[10]), false, false)[0]);
    const float mown = mq[0];
    lds_wait(sync + (qg ? 3 : 1), 4);

    {
        LAS const char* s = lds + A_XO + kq * 3 * A_SLOT;
        f32x4 e[3], u[3], v[3]; float M = mown;
#pragma unroll
        for (int w = 0; w < 3; ++w) {
            e[w] = *(LAS const f32x4*)(s + w * A_SLOT + 2048 + r * 16);
            u[w] = *(LAS const f32x4*)(s + w * A_SLOT + lane * 16); v[w] = *(LAS const f32x4*)(s + w * A_SLOT + 1024 + lane * 16);
        }
        asm volatile("s_waitcnt lgkmcnt(0)" ::: "memory");
        if (qg == 1 && lane == 0) __hip_atomic_fetch_add(sync + 2, 1u, __ATOMIC_RELAXED, __HIP_MEMORY_SCOPE_WORKGROUP);
#pragma unroll
        for (int w = 0; w < 3; ++w) M = fmaxf(M, e[w][3]);
        const float w0 = __builtin_amdgcn_exp2f(mown - M);
        float a[8], d16 = w0 * o16, d17 = w0 * o17, l = w0 * ol;
#pragma unroll
        for (int k = 0; k < 8; ++k) a[k] = w0 * O0[k];
#pragma unroll
        for (int w = 0; w < 3; ++w) {
            const float wt = __builtin_amdgcn_exp2f(e[w][3] - M);
#pragma unroll
            for (int k = 0; k < 4; ++k) { a[k] += wt * u[w][k]; a[4 + k] += wt * v[w][k]; }
            d16 += wt * e[w][0]; d17 += wt * e[w][1]; l += wt * e[w][2];
        }
        const float il = __builtin_amdgcn_rcpf(l);
        const int t = (qb * 8 + qg * 4 + kq) * 32 + r, tt = t >> 4, tr = t & 15;
        char* of = p.ws + WS_OF + (size_t)tt * 18 * 1024 + tr * 16;
        const auto s0 = __builtin_amdgcn_permlane32_swap(pkbf(a[0] * il, a[1] * il), pkbf(a[4] * il, a[5] * il), false, false);
        const auto s1 = __builtin_amdgcn_permlane32_swap(pkbf(a[2] * il, a[3] * il), pkbf(a[6] * il, a[7] * il), false, false);
        st16(of + ((head >> 1) * 4 + 2 * (head & 1) + hh) * 256, u32x4{s0[0], s1[0], s0[1], s1[1]});
        if (hh == 0) st4(of + ((16 + (head >> 4)) * 4 + ((head >> 2) & 3)) * 256 + (head & 3) * 4, pkbf(d16 * il, d17 * il));
    }

    auto load_wo = [&](int j0, int step) {
        const char* wob = (const char*)p.wo + jb * 144;
#pragma unroll 1
        for (int j = j0; j < 9; j += step) {
            const int c = j * 64 + lane, rho = (c * 7282) >> 16, cc = c - 9 * rho;
            const int kp = (rho & ~12) | ((rho & 4) << 1) | ((rho & 8) >> 1), gk = kp >> 3, e = kp & 7;
            const unsigned vm = (18 * (gk >> 1) + 8 * (gk & 1) + e) * 1152 + cc * 16, ve = (72 * gk + 18 * (e >> 1) + 16 + (e & 1)) * 1152 + cc * 16;
#pragma unroll
            for (int blk = 0; blk < 8; ++blk) {
                dma16(vm, wob + blk * 72 * 1152, ldsb + T_WO + blk * 9216 + j * 1024);
            }
            dma16(ve, wob, ldsb + T_WO + 8 * 9216 + j * 1024);
        }
    };
    if (qg == 1) {
#pragma unroll
        for (int i = 0; i < 3; ++i) {
            const int inst = kq + 4 * i;
            if (inst < 9) {
                const int c = inst * 64 + lane, row = (c * 7282) >> 16;
                dma16(row * 1152 + (c - 9 * row) * 16, (const char*)p.x + (size_t)rbk * 64 * 1152 + jb * 144, ldsb + T_X + inst * 1024);
            }
        }
        if (kq == 1) dma16m(lane * 16, p.ws + WS_GATE + rbk * 256, ldsb + T_G, 0xffffull);
        if (kq == 2) dma16m(lane * 16, (const char*)p.dskip + jb * 144, ldsb + T_D, 0x1ffull);
    }
    gu32* cnt = (gu32*)(p.ws + WS_CNT);
    if (qg == 1) asm volatile("s_waitcnt vmcnt(0)" ::: "memory");
    if (lane == 0 && __hip_atomic_fetch_add(sync + 4 + 2 * (1 - qg), 1u, __ATOMIC_RELAXED, __HIP_MEMORY_SCOPE_WORKGROUP) == 3u)
        __hip_atomic_fetch_add(cnt + ((b >> 5) + 8 * qg) * 64, 1u, __ATOMIC_RELAXED, __HIP_MEMORY_SCOPE_AGENT);
    if (qg == 1) {
        __builtin_amdgcn_s_setprio(0);
        load_wo(kq, 4);
        lds_arrive(sync + 5, lane);
    }
    lds_wait(sync + 5, 4);

    bf16x8 aw[6][5];
    {
        LAS const char* wt = lds + T_WO + (9 * qg * 32 + 16 * ((lane >> 4) >> 1) + 4 * ((lane >> 4) & 1) + ((lane & 15) >> 2)) * 144 + (lane & 3) * 8;
#pragma unroll
        for (int ks = 0; ks < 6; ++ks)
#pragma unroll
            for (int m = 0; m < 5; ++m) { LAS const char* s = wt + ks * 32 * 144 + m * 32; aw[ks][m] = cat8(trread(s), trread(s + 8 * 144)); }
    }
    if (wave == 0) {
        for (unsigned spins = 0; spins < SPIN_CAP; ++spins) {
            const unsigned c0 = __hip_atomic_load(cnt + ((b & 7) + (rbk & 2) * 4) * 64, __ATOMIC_RELAXED, __HIP_MEMORY_SCOPE_AGENT);
            const unsigned c1 = __hip_atomic_load(cnt + (8 + jb) * 64, __ATOMIC_RELAXED, __HIP_MEMORY_SCOPE_AGENT);
            if (rfl((int)(c0 >= 32u && c1 >= 32u))) break;
            __builtin_amdgcn_s_sleep(1);
        }
    }
    asm volatile("s_barrier" ::: "memory");

    if (qg == 0) out_tail<0>(p, lds, rbk, jb, kq, lane, aw); else out_tail<1>(p, lds, rbk, jb, kq, lane, aw);

}
#if defined(__HIP_DEVICE_COMPILE__)
#pragma clang attribute pop
#endif
extern "C" void kernel_launch(void* const* d_in, const int* in_sizes, int n_in, void* d_out, int out_size, void* d_ws, size_t ws_size,
                              hipStream_t stream) {
    Params p;
    p.x = (const unsigned short*)d_in[0]; p.logA = (const unsigned short*)d_in[1]; p.Aim = (const unsigned short*)d_in[2];
    p.Cre = (const unsigned short*)d_in[3]; p.Cim = (const unsigned short*)d_in[4]; p.logdt = (const unsigned short*)d_in[5];
    p.dskip = (const unsigned short*)d_in[6]; p.wqkv = (const unsigned short*)d_in[8]; p.wo = (const unsigned short*)d_in[9];
    p.gatew = (const unsigned short*)d_in[10]; p.out = (unsigned short*)d_out; p.ws = (char*)d_ws;
    hipFuncSetAttribute((const void*)k1_proj, hipFuncAttributeMaxDynamicSharedMemorySize, K1_LDS);
    hipFuncSetAttribute((const void*)k2_attn, hipFuncAttributeMaxDynamicSharedMemorySize, K2_LDS);
    k1_proj<<<512, 512, K1_LDS, stream>>>(p);
    k2_attn<<<256, 512, K2_LDS, stream>>>(p);
}
```
